```python
import torch

def _softmax(x, axis=-1):
    return torch.softmax(x, dim=axis)
def _gelu(x):
    return torch.nn.functional.gelu(x, approximate="tanh")
def _silu(x):
    return torch.nn.functional.silu(x)
def _sigmoid(x):
    return torch.sigmoid(x)
def _layernorm(x, axis=-1, eps=1e-5):
    m = x.mean(dim=axis, keepdim=True)
    v = x.var(dim=axis, keepdim=True, unbiased=False)
    return (x - m) / torch.sqrt(v + eps)
def _scatter_add(tgt, idx, src, axis=0):
    return tgt.clone().index_add_(axis, idx, src)
def _segment_sum(src, seg, n):
    out = torch.zeros((n,) + src.shape[1:], dtype=src.dtype)
    return out.index_add_(0, seg, src)
def _pad(x, pw):
    flat = tuple(v for pair in reversed(pw) for v in pair)
    return torch.nn.functional.pad(x, flat)

kernel_name = 'standalone__moe_arch_mod__decode'

def setup_inputs():

    _w = (lambda a: (lambda t: t.to(torch.bfloat16) if t.is_floating_point() else t)(torch.as_tensor(a)))
    B, S, D, F, C = 8, 1, 1024, 3584, 1
    return {
        "x": _w((lambda *s: torch.randn(*s, dtype=torch.bfloat16) * 0.2)(B, S, D)),
        "w_r": _w((lambda *s: torch.randn(*s, dtype=torch.bfloat16) * 0.2)(D)),
        "w_gate": _w((lambda *s: torch.randn(*s, dtype=torch.bfloat16) * 0.1)(D, F)),
        "w_up": _w((lambda *s: torch.randn(*s, dtype=torch.bfloat16) * 0.1)(D, F)),
        "w_down": _w((lambda *s: torch.randn(*s, dtype=torch.bfloat16) * 0.1)(F, D)),
    }

def reference(x, w_r, w_gate, w_up, w_down):
    B, S, D = x.shape
    C = 1
    r = x @ w_r
    idx = torch.argsort(-r, axis=-1)[:, :C]
    idx_e = (lambda x, n, axis: torch.repeat_interleave(x, n, dim=axis))(idx.reshape(B, C, 1), D, axis=2)
    x_sel = (lambda x, i, axis: torch.take_along_dim(x, i, dim=axis))(x, idx_e, axis=1)
    g = _silu(x_sel @ w_gate)
    u = x_sel @ w_up
    y = (g * u) @ w_down
    r_sel = _sigmoid((lambda x, i, axis: torch.take_along_dim(x, i, dim=axis))(r, idx, axis=1))
    y = y * r_sel.reshape(B, C, 1)
    b_off = (torch.arange(B) * S).reshape(B, 1)
    flat_idx = (idx + b_off).reshape(B * C)
    zero = (x * 0.0).reshape(B * S, D)
    upd = _scatter_add(zero, flat_idx, y.reshape(B * C, D), axis=0)
    return x + upd.reshape(B, S, D)
```

```cpp
#include <hip/hip_runtime.h>
#include <hip/hip_bf16.h>
#include <stdint.h>

typedef unsigned short u16;
__device__ __forceinline__ float bf2f(u16 v){ return __uint_as_float(((unsigned)v)<<16); }
__device__ __forceinline__ u16 f2bf(float f){ __bf16 b = (__bf16)f; return __builtin_bit_cast(u16,b); }
__device__ __forceinline__ float rbf(float f){ return bf2f(f2bf(f)); }

constexpr int B=8, D=1024, F=3584;

__global__ void __launch_bounds__(256) k1(const u16* x, const u16* wg, const u16* wu, u16* h){
  __shared__ float xs[B][D];
  __shared__ float red[2][16][B][16];
  int t=threadIdx.x;
  for(int i=t;i<B*D;i+=256) xs[i/D][i%D]=bf2f(x[i]);
  __syncthreads();
  int c=t&15, kg=t>>4;
  int f=blockIdx.x*16+c;
  float g[B],u[B];
  for(int b=0;b<B;b++){g[b]=0;u[b]=0;}
  for(int k=kg;k<D;k+=16){
    float a=bf2f(wg[(size_t)k*F+f]), w=bf2f(wu[(size_t)k*F+f]);
    for(int b=0;b<B;b++){ g[b]+=xs[b][k]*a; u[b]+=xs[b][k]*w; }
  }
  for(int b=0;b<B;b++){ red[0][kg][b][c]=g[b]; red[1][kg][b][c]=u[b]; }
  __syncthreads();
  if(t<128){
    int b=t>>4;
    float gg=0,uu=0;
    for(int i=0;i<16;i++){ gg+=red[0][i][b][c]; uu+=red[1][i][b][c]; }
    gg=rbf(gg); uu=rbf(uu);
    float s=rbf(gg/(1.f+__expf(-gg)));
    h[b*F+f]=f2bf(s*uu);
  }
}
__global__ void __launch_bounds__(256) k2(const u16* x, const u16* wr, const u16* wd, const u16* h, u16* out){
  __shared__ float red[16][B][16];
  __shared__ float rs[B];
  int t=threadIdx.x;
  int c=t&15, kg=t>>4;
  int n=blockIdx.x*16+c;
  float y[B];
  for(int b=0;b<B;b++) y[b]=0;
  for(int k=kg;k<F;k+=16){
    float a=bf2f(wd[(size_t)k*D+n]);
    for(int b=0;b<B;b++) y[b]+=bf2f(h[b*F+k])*a;
  }
  for(int b=0;b<B;b++) red[kg][b][c]=y[b];
  if(t<B){ float r=0; for(int k=0;k<D;k++) r+=bf2f(x[t*D+k])*bf2f(wr[k]); r=rbf(r); rs[t]=rbf(1.f/(1.f+__expf(-r))); }
  __syncthreads();
  if(t<128){
    int b=t>>4;
    float yy=0;
    for(int i=0;i<16;i++) yy+=red[i][b][c];
    yy=rbf(yy);
    yy=rbf(yy*rs[b]);
    float xv=bf2f(x[b*D+n]);
    float upd=rbf(rbf(xv*0.0f)+yy);
    out[b*D+n]=f2bf(xv+upd);
  }
}

extern "C" void kernel_launch(void* const* d_in, const int* in_sizes, int n_in,
                              void* d_out, int out_size, void* d_ws, size_t ws_size,
                              hipStream_t stream) {
  const u16* x=(const u16*)d_in[0]; const u16* wr=(const u16*)d_in[1];
  const u16* wg=(const u16*)d_in[2]; const u16* wu=(const u16*)d_in[3]; const u16* wd=(const u16*)d_in[4];
  u16* h=(u16*)d_ws;
  k1<<<F/16,256,0,stream>>>(x,wg,wu,h);
  k2<<<D/16,256,0,stream>>>(x,wr,wd,h,(u16*)d_out);
}
```

```cpp
#include <hip/hip_runtime.h>
#include <stdint.h>

typedef unsigned v4u __attribute__((ext_vector_type(4)));
typedef unsigned v2u __attribute__((ext_vector_type(2)));
typedef float f32x4 __attribute__((ext_vector_type(4)));
typedef __bf16 bf16x8 __attribute__((ext_vector_type(8)));
typedef __bf16 bf16x2 __attribute__((ext_vector_type(2)));
typedef float f32x2 __attribute__((ext_vector_type(2)));
#define LAS __attribute__((address_space(3)))

#define WAUX 0
constexpr int D = 1024, F = 3584;
constexpr int NWG = 224;
constexpr int AUX_SC1 = 16;
constexpr int P1_OFF = 4096, P2_OFF = P1_OFF + NWG * 2048, WS_BYTES = P2_OFF + 256 * 7168;
constexpr unsigned SPIN_CAP = 1u << 16;

__device__ __forceinline__ __amdgpu_buffer_rsrc_t mkrs(const void* p, int bytes) {
  return __builtin_amdgcn_make_buffer_rsrc((void*)p, 0, bytes, 0x00020000);
}
__device__ __forceinline__ float asf(unsigned u) { return __builtin_bit_cast(float, u); }
__device__ __forceinline__ unsigned asu(float f) { return __builtin_bit_cast(unsigned, f); }
__device__ __forceinline__ unsigned pk_bf16(float a, float b) {
  f32x2 v = {a, b};
  return __builtin_bit_cast(unsigned, __builtin_convertvector(v, bf16x2));
}
__device__ __forceinline__ float rbf(float a) { return asf(pk_bf16(a, a) << 16); }
__device__ __forceinline__ float ror8(float v) {
  return asf(__builtin_amdgcn_update_dpp(0u, asu(v), 0x128, 0xf, 0xf, true));
}
__device__ __forceinline__ void tile_mma(const v4u (&a)[8], v4u xf, bool hi, float (&o)[8]) {
#pragma unroll
  for (int cc = 0; cc < 8; ++cc) {
    const unsigned sel = (cc & 1) ? 0x07060302u : 0x05040100u;
    v4u b;
    b.x = __builtin_amdgcn_perm(a[1][cc >> 1], a[0][cc >> 1], sel);
    b.y = __builtin_amdgcn_perm(a[3][cc >> 1], a[2][cc >> 1], sel);
    b.z = __builtin_amdgcn_perm(a[5][cc >> 1], a[4][cc >> 1], sel);
    b.w = __builtin_amdgcn_perm(a[7][cc >> 1], a[6][cc >> 1], sel);
    f32x4 z = {0.f, 0.f, 0.f, 0.f};
    f32x4 d = __builtin_amdgcn_mfma_f32_16x16x32_bf16(__builtin_bit_cast(bf16x8, xf), __builtin_bit_cast(bf16x8, b), z, 0, 0, 0);
    float own = hi ? d[3] : d[0];
    float oth = hi ? d[1] : d[2];
    o[cc] = own + ror8(oth);
  }
}
__device__ __forceinline__ v4u tag4(f32x4 v, unsigned T) {
  v4u r;
  r.x = (asu(v[0]) & ~0xFFu) | (T & 0xFF);
  r.y = (asu(v[1]) & ~0xFFu) | ((T >> 8) & 0xFF);
  r.z = (asu(v[2]) & ~0xFFu) | ((T >> 16) & 0xFF);
  r.w = (asu(v[3]) & ~0xFFu) | (T >> 24);
  return r;
}
__device__ __forceinline__ v4u untag4(v4u v, unsigned T) {
  v4u r;
  r.x = v.x ^ (T & 0xFF);
  r.y = v.y ^ ((T >> 8) & 0xFF);
  r.z = v.z ^ ((T >> 16) & 0xFF);
  r.w = v.w ^ (T >> 24);
  return r;
}

__global__ void __launch_bounds__(512, 2) moe_decode(const void* x, const void* wr, const void* wg, const void* wu,
                                                     const void* wd, void* out, void* ws) {
  __shared__ __attribute__((aligned(16))) unsigned char smem[84 * 1024];
  LAS unsigned char* L = (LAS unsigned char*)smem;
  const int l = threadIdx.x & 63, w = __builtin_amdgcn_readfirstlane(threadIdx.x >> 6);
  const int b = blockIdx.x;
  const int bb = (b & 7) * 28 + (b >> 3);
  const int grp = bb >> 2, m = bb & 3;
  const int q = l >> 4, r = (l >> 3) & 1, nb = l & 7;
  const bool hi = r != 0;

  const __amdgpu_buffer_rsrc_t rsW = mkrs((m & 1) ? wu : wg, D * F * 2);
  const __amdgpu_buffer_rsrc_t rsD = mkrs(wd, D * F * 2);
  const __amdgpu_buffer_rsrc_t rsX = mkrs(x, 8 * D * 2);
  const __amdgpu_buffer_rsrc_t rsS = mkrs(ws, WS_BYTES);

  const int k0 = 512 * (m >> 1) + 64 * w;
  v4u a[8];
  {
    const int voff = (32 * r + 8 * q) * (F * 2) + nb * 16;
    const int soff = k0 * (F * 2) + grp * 128;
#pragma unroll
    for (int j = 0; j < 8; ++j) a[j] = __builtin_amdgcn_raw_buffer_load_b128(rsW, voff, soff + j * (F * 2), WAUX);
  }
  const v4u xf = __builtin_amdgcn_raw_buffer_load_b128(rsX, ((l & 15) >> 1) * 2048 + (l & 1) * 64 + q * 16, k0 * 2, 0);
  const unsigned Ev = __builtin_amdgcn_raw_buffer_load_b32(rsS, 0, 0, AUX_SC1);
  v4u e[8];
  v4u xr0, xr1, wr0, wr1;
  v2u xres;
  if (w >= 4) {
    const int voff = (32 * r + 8 * q) * (D * 2) + nb * 16;
    const int soff = grp * 64 * (D * 2) + m * 512 + (w - 4) * 128;
#pragma unroll
    for (int j = 0; j < 8; ++j) e[j] = __builtin_amdgcn_raw_buffer_load_b128(rsD, voff, soff + j * (D * 2), WAUX);
  } else if (w >= 2 && b < 128) {
    const __amdgpu_buffer_rsrc_t rsR = mkrs(wr, D * 2);
    const int tok = b & 7, cb = b >> 3;
    xr0 = __builtin_amdgcn_raw_buffer_load_b128(rsX, l * 16, tok * 2048, 0);
    xr1 = __builtin_amdgcn_raw_buffer_load_b128(rsX, l * 16, tok * 2048 + 1024, 0);
    wr0 = __builtin_amdgcn_raw_buffer_load_b128(rsR, l * 16, 0, 0);
    wr1 = __builtin_amdgcn_raw_buffer_load_b128(rsR, l * 16, 1024, 0);
    xres = __builtin_amdgcn_raw_buffer_load_b64(rsX, nb * 16, tok * 2048 + cb * 128 + (w - 2) * 8, 0);
  }
  __builtin_amdgcn_sched_barrier(0);

  float o[8];
  tile_mma(a, xf, hi, o);
  const unsigned T = __builtin_amdgcn_readfirstlane(Ev) + 0x01010101u;
  {
    LAS f32x4* red = (LAS f32x4*)L;
    red[w * 128 + l] = f32x4{o[0], o[1], o[2], o[3]};
    red[w * 128 + 64 + l] = f32x4{o[4], o[5], o[6], o[7]};
  }
  asm volatile("s_waitcnt lgkmcnt(0)" ::: "memory");
  __builtin_amdgcn_s_barrier();
  asm volatile("" ::: "memory");
  float rsel = 0.f;
  if (w < 2) {
    LAS f32x4* red = (LAS f32x4*)L;
    f32x4 s = red[w * 64 + l];
#pragma unroll
    for (int i = 1; i < 8; ++i) s += red[i * 128 + w * 64 + l];
    __builtin_amdgcn_raw_buffer_store_b128(tag4(s, T), rsS, l * 16, P1_OFF + bb * 2048 + w * 1024, AUX_SC1);
    v4u p[4];
    for (unsigned spin = 0; spin < SPIN_CAP; ++spin) {
      unsigned chk = 0;
#pragma unroll
      for (int i = 0; i < 4; ++i) p[i] = __builtin_amdgcn_raw_buffer_load_b128(rsS, l * 16, P1_OFF + (grp * 4 + i) * 2048 + w * 1024, AUX_SC1);
#pragma unroll
      for (int i = 0; i < 4; ++i) {
        p[i] = untag4(p[i], T);
        chk |= p[i].x | p[i].y | p[i].z | p[i].w;
      }
      if (__builtin_amdgcn_ballot_w64((chk & 0xFF) != 0) == 0) break;
      asm volatile("" ::: "memory");
    }
    float h[4];
#pragma unroll
    for (int i = 0; i < 4; ++i) {
      const float g = rbf(asf(p[0][i]) + asf(p[2][i]));
      const float u = rbf(asf(p[1][i]) + asf(p[3][i]));
      const float s1 = rbf(g * __builtin_amdgcn_rcpf(1.0f + __expf(-g)));
      h[i] = s1 * u;
    }
    v2u hv = {pk_bf16(h[0], h[1]), pk_bf16(h[2], h[3])};
    *(LAS v2u*)(L + 16384 + l * 16 + w * 8) = hv;
  } else if (w < 4 && b < 128) {
    float acc = 0.f;
#pragma unroll
    for (int i = 0; i < 4; ++i) {
      acc = fmaf(asf(xr0[i] << 16), asf(wr0[i] << 16), acc);
      acc = fmaf(asf(xr0[i] & 0xFFFF0000u), asf(wr0[i] & 0xFFFF0000u), acc);
      acc = fmaf(asf(xr1[i] << 16), asf(wr1[i] << 16), acc);
      acc = fmaf(asf(xr1[i] & 0xFFFF0000u), asf(wr1[i] & 0xFFFF0000u), acc);
    }
#pragma unroll
    for (int s = 1; s < 64; s <<= 1) acc += __shfl_xor(acc, s);
    const float rr = rbf(acc);
    rsel = rbf(__builtin_amdgcn_rcpf(1.0f + __expf(-rr)));
  }
  asm volatile("s_waitcnt lgkmcnt(0)" ::: "memory");
  __builtin_amdgcn_s_barrier();
  asm volatile("" ::: "memory");
  if (w >= 4) {
    const v4u hf = *(LAS v4u*)(L + 16384 + ((l & 15) >> 1) * 128 + ((l & 1) * 4 + q) * 16);
    float y[8];
    tile_mma(e, hf, hi, y);
    const int voff = (l >> 3) * (2 * 7168) + nb * 16;
    const int soff = P2_OFF + (m * 4 + (w - 4)) * (16 * 7168) + grp * 128;
    __builtin_amdgcn_raw_buffer_store_b128(tag4(f32x4{y[0], y[1], y[2], y[3]}, T), rsS, voff, soff, AUX_SC1);
    __builtin_amdgcn_raw_buffer_store_b128(tag4(f32x4{y[4], y[5], y[6], y[7]}, T), rsS, voff, soff + 7168, AUX_SC1);
  } else if (w >= 2 && b < 128) {
    const int tok = b & 7, cb = b >> 3;
    const int soff = P2_OFF + ((cb * 8 + tok) * 2 + (w - 2)) * 7168;
    v4u p[7];
    for (unsigned spin = 0; spin < SPIN_CAP; ++spin) {
      unsigned chk = 0;
#pragma unroll
      for (int i = 0; i < 7; ++i) p[i] = __builtin_amdgcn_raw_buffer_load_b128(rsS, l * 16, soff + i * 1024, AUX_SC1);
#pragma unroll
      for (int i = 0; i < 7; ++i) {
        p[i] = untag4(p[i], T);
        chk |= p[i].x | p[i].y | p[i].z | p[i].w;
      }
      if (__builtin_amdgcn_ballot_w64((chk & 0xFF) != 0) == 0) break;
      asm volatile("" ::: "memory");
    }
    f32x4 s = __builtin_bit_cast(f32x4, p[0]);
#pragma unroll
    for (int i = 1; i < 7; ++i) s += __builtin_bit_cast(f32x4, p[i]);
    float res[4];
#pragma unroll
    for (int i = 0; i < 4; ++i) {
      float v = s[i];
      v += ror8(v);
      v += __shfl_xor(v, 16);
      v += __shfl_xor(v, 32);
      const float xv = (i & 1) ? asf(xres[i >> 1] & 0xFFFF0000u) : asf(xres[i >> 1] << 16);
      const float yy = rbf(rbf(v) * rsel);
      const float upd = rbf(rbf(xv * 0.0f) + yy);
      res[i] = xv + upd;
    }
    if (l < 8) {
      const __amdgpu_buffer_rsrc_t rsO = mkrs(out, 8 * D * 2);
      v2u ov = {pk_bf16(res[0], res[1]), pk_bf16(res[2], res[3])};
      __builtin_amdgcn_raw_buffer_store_b64(ov, rsO, nb * 16, tok * 2048 + cb * 128 + (w - 2) * 8, 0);
    }
    if (b == 0 && w == 2 && l == 0) __builtin_amdgcn_raw_buffer_store_b32(T, rsS, 0, 0, AUX_SC1);
  }
}

extern "C" void kernel_launch(void* const* d_in, const int* in_sizes, int n_in, void* d_out, int out_size, void* d_ws,
                              size_t ws_size, hipStream_t stream) {
  moe_decode<<<NWG, 512, 0, stream>>>(d_in[0], d_in[1], d_in[2], d_in[3], d_in[4], d_out, d_ws);
}
```

```cpp
#define SJ 0
#define SMAX 1
#define SK 0
#include <hip/hip_runtime.h>
#include <stdint.h>

typedef unsigned v4u __attribute__((ext_vector_type(4)));
typedef unsigned v2u __attribute__((ext_vector_type(2)));
typedef float f32x4 __attribute__((ext_vector_type(4)));
typedef __bf16 bf16x8 __attribute__((ext_vector_type(8)));
typedef __bf16 bf16x2 __attribute__((ext_vector_type(2)));
typedef float f32x2 __attribute__((ext_vector_type(2)));
#define LAS __attribute__((address_space(3)))

#define WAUX 2
constexpr int D = 1024, F = 3584;
constexpr int NWG = 224;
constexpr int AUX_SC1 = 16;
constexpr int P1_OFF = 4096, P2_OFF = P1_OFF + NWG * 2048, WS_BYTES = P2_OFF + 256 * 7168;
constexpr unsigned SPIN_CAP = 1u << 16;

__device__ __forceinline__ __amdgpu_buffer_rsrc_t mkrs(const void* p, int bytes) {
  return __builtin_amdgcn_make_buffer_rsrc((void*)p, 0, bytes, 0x00020000);
}
__device__ __forceinline__ float asf(unsigned u) { return __builtin_bit_cast(float, u); }
__device__ __forceinline__ unsigned asu(float f) { return __builtin_bit_cast(unsigned, f); }
__device__ __forceinline__ unsigned pk_bf16(float a, float b) {
  f32x2 v = {a, b};
  return __builtin_bit_cast(unsigned, __builtin_convertvector(v, bf16x2));
}
__device__ __forceinline__ float rbf(float a) { return asf(pk_bf16(a, a) << 16); }
__device__ __forceinline__ float ror8(float v) {
  return asf(__builtin_amdgcn_update_dpp(0u, asu(v), 0x128, 0xf, 0xf, true));
}
__device__ __forceinline__ void tile_mma(const v4u (&a)[8], v4u xf, bool hi, float (&o)[8]) {
#pragma unroll
  for (int cc = 0; cc < 8; ++cc) {
    const unsigned sel = (cc & 1) ? 0x07060302u : 0x05040100u;
    v4u b;
    b.x = __builtin_amdgcn_perm(a[1][cc >> 1], a[0][cc >> 1], sel);
    b.y = __builtin_amdgcn_perm(a[3][cc >> 1], a[2][cc >> 1], sel);
    b.z = __builtin_amdgcn_perm(a[5][cc >> 1], a[4][cc >> 1], sel);
    b.w = __builtin_amdgcn_perm(a[7][cc >> 1], a[6][cc >> 1], sel);
    f32x4 z = {0.f, 0.f, 0.f, 0.f};
    f32x4 d = __builtin_amdgcn_mfma_f32_16x16x32_bf16(__builtin_bit_cast(bf16x8, xf), __builtin_bit_cast(bf16x8, b), z, 0, 0, 0);
    float own = hi ? d[3] : d[0];
    float oth = hi ? d[1] : d[2];
    o[cc] = own + ror8(oth);
  }
}
__device__ __forceinline__ v4u tag4(f32x4 v, unsigned T) {
  v4u r;
  r.x = (asu(v[0]) & ~0xFFu) | (T & 0xFF);
  r.y = (asu(v[1]) & ~0xFFu) | ((T >> 8) & 0xFF);
  r.z = (asu(v[2]) & ~0xFFu) | ((T >> 16) & 0xFF);
  r.w = (asu(v[3]) & ~0xFFu) | (T >> 24);
  return r;
}
__device__ __forceinline__ v4u untag4(v4u v, unsigned T) {
  v4u r;
  r.x = v.x ^ (T & 0xFF);
  r.y = v.y ^ ((T >> 8) & 0xFF);
  r.z = v.z ^ ((T >> 16) & 0xFF);
  r.w = v.w ^ (T >> 24);
  return r;
}

#define STAMP_OFF (8<<20)
__device__ __forceinline__ unsigned long long rt(){ unsigned long long t; asm volatile("s_memrealtime %0\n\ts_waitcnt lgkmcnt(0)":"=s"(t)::"memory"); return t; }
#define ST(i) { __builtin_amdgcn_sched_barrier(0); ts[i]=rt(); __builtin_amdgcn_sched_barrier(0); }
__global__ void __launch_bounds__(512, 2) moe_decode(const void* x, const void* wr, const void* wg, const void* wu,
                                                     const void* wd, void* out, void* ws) {
  __shared__ __attribute__((aligned(16))) unsigned char smem[84 * 1024];
  unsigned long long ts[8]={0,0,0,0,0,0,0,0};
  ST(0)
  LAS unsigned char* L = (LAS unsigned char*)smem;
  const int l = threadIdx.x & 63, w = __builtin_amdgcn_readfirstlane(threadIdx.x >> 6);
  const int b = blockIdx.x;
  const int bb = (b & 7) * 28 + (b >> 3);
  const int grp = bb >> 2, m = bb & 3;
  const int q = l >> 4, r = (l >> 3) & 1, nb = l & 7;
  const bool hi = r != 0;

  const __amdgpu_buffer_rsrc_t rsW = mkrs((m & 1) ? wu : wg, D * F * 2);
  const __amdgpu_buffer_rsrc_t rsD = mkrs(wd, D * F * 2);
  const __amdgpu_buffer_rsrc_t rsX = mkrs(x, 8 * D * 2);
  const __amdgpu_buffer_rsrc_t rsS = mkrs(ws, WS_BYTES);

  const int k0 = 512 * (m >> 1) + 64 * w;
  v4u a[8];
  {
    const int voff = (32 * r + 8 * q) * (F * 2) + nb * 16;
    const int soff = k0 * (F * 2) + grp * 128;
#pragma unroll
    for (int j = 0; j < 8; ++j) a[j] = __builtin_amdgcn_raw_buffer_load_b128(rsW, voff, soff + j * (F * 2), WAUX);
  }
  const v4u xf = __builtin_amdgcn_raw_buffer_load_b128(rsX, ((l & 15) >> 1) * 2048 + (l & 1) * 64 + q * 16, k0 * 2, 0);
  const unsigned Ev = __builtin_amdgcn_raw_buffer_load_b32(rsS, 0, 0, AUX_SC1);
  v4u e[8];
  v4u xr0, xr1, wr0, wr1;
  v2u xres;
  if (w >= 4) {
    const int voff = (32 * r + 8 * q) * (D * 2) + nb * 16;
    const int soff = grp * 64 * (D * 2) + m * 512 + (w - 4) * 128;
#pragma unroll
    for (int j = 0; j < 8; ++j) e[j] = __builtin_amdgcn_raw_buffer_load_b128(rsD, voff, soff + j * (D * 2), WAUX);
  } else if (w >= 2 && b < 128) {
    const __amdgpu_buffer_rsrc_t rsR = mkrs(wr, D * 2);
    const int tok = b & 7, cb = b >> 3;
    xr0 = __builtin_amdgcn_raw_buffer_load_b128(rsX, l * 16, tok * 2048, 0);
    xr1 = __builtin_amdgcn_raw_buffer_load_b128(rsX, l * 16, tok * 2048 + 1024, 0);
    wr0 = __builtin_amdgcn_raw_buffer_load_b128(rsR, l * 16, 0, 0);
    wr1 = __builtin_amdgcn_raw_buffer_load_b128(rsR, l * 16, 1024, 0);
    xres = __builtin_amdgcn_raw_buffer_load_b64(rsX, nb * 16, tok * 2048 + cb * 128 + (w - 2) * 8, 0);
  }
  __builtin_amdgcn_sched_barrier(0);

  ST(1)
  float o[8];
  tile_mma(a, xf, hi, o);
  const unsigned T = __builtin_amdgcn_readfirstlane(Ev) + 0x01010101u;
  {
    LAS f32x4* red = (LAS f32x4*)L;
    red[w * 128 + l] = f32x4{o[0], o[1], o[2], o[3]};
    red[w * 128 + 64 + l] = f32x4{o[4], o[5], o[6], o[7]};
  }
  asm volatile("s_waitcnt lgkmcnt(0)" ::: "memory");
  __builtin_amdgcn_s_barrier();
  asm volatile("" ::: "memory");
  ST(2)
  float rsel = 0.f;
  if (w < 2) {
    LAS f32x4* red = (LAS f32x4*)L;
    f32x4 s = red[w * 64 + l];
#pragma unroll
    for (int i = 1; i < 8; ++i) s += red[i * 128 + w * 64 + l];
    __builtin_amdgcn_raw_buffer_store_b128(tag4(s, T), rsS, l * 16, P1_OFF + bb * 2048 + w * 1024, AUX_SC1);
    ST(3)
    v4u p[4];
    for (unsigned spin = 0; spin < SPIN_CAP; ++spin) {
      unsigned chk = 0;
#pragma unroll
      for (int i = 0; i < 4; ++i) p[i] = __builtin_amdgcn_raw_buffer_load_b128(rsS, l * 16, P1_OFF + (grp * 4 + i) * 2048 + w * 1024, AUX_SC1);
#pragma unroll
      for (int i = 0; i < 4; ++i) {
        p[i] = untag4(p[i], T);
        chk |= p[i].x | p[i].y | p[i].z | p[i].w;
      }
      if (__builtin_amdgcn_ballot_w64((chk & 0xFF) != 0) == 0) break;
      asm volatile("" ::: "memory");
    }
    ST(4)
    float h[4];
#pragma unroll
    for (int i = 0; i < 4; ++i) {
      const float g = rbf(asf(p[0][i]) + asf(p[2][i]));
      const float u = rbf(asf(p[1][i]) + asf(p[3][i]));
      const float s1 = rbf(g * __builtin_amdgcn_rcpf(1.0f + __expf(-g)));
      h[i] = s1 * u;
    }
    v2u hv = {pk_bf16(h[0], h[1]), pk_bf16(h[2], h[3])};
    *(LAS v2u*)(L + 16384 + l * 16 + w * 8) = hv;
  } else if (w < 4 && b < 128) {
    float acc = 0.f;
#pragma unroll
    for (int i = 0; i < 4; ++i) {
      acc = fmaf(asf(xr0[i] << 16), asf(wr0[i] << 16), acc);
      acc = fmaf(asf(xr0[i] & 0xFFFF0000u), asf(wr0[i] & 0xFFFF0000u), acc);
      acc = fmaf(asf(xr1[i] << 16), asf(wr1[i] << 16), acc);
      acc = fmaf(asf(xr1[i] & 0xFFFF0000u), asf(wr1[i] & 0xFFFF0000u), acc);
    }
#pragma unroll
    for (int s = 1; s < 64; s <<= 1) acc += __shfl_xor(acc, s);
    const float rr = rbf(acc);
    rsel = rbf(__builtin_amdgcn_rcpf(1.0f + __expf(-rr)));
  }
  asm volatile("s_waitcnt lgkmcnt(0)" ::: "memory");
  __builtin_amdgcn_s_barrier();
  asm volatile("" ::: "memory");
  ST(5)
  if (w >= 4) {
    const v4u hf = *(LAS v4u*)(L + 16384 + ((l & 15) >> 1) * 128 + ((l & 1) * 4 + q) * 16);
    float y[8];
    tile_mma(e, hf, hi, y);
    const int voff = (l >> 3) * (2 * 7168) + nb * 16;
    const int soff = P2_OFF + (m * 4 + (w - 4)) * (16 * 7168) + grp * 128;
    __builtin_amdgcn_raw_buffer_store_b128(tag4(f32x4{y[0], y[1], y[2], y[3]}, T), rsS, voff, soff, AUX_SC1);
    __builtin_amdgcn_raw_buffer_store_b128(tag4(f32x4{y[4], y[5], y[6], y[7]}, T), rsS, voff, soff + 7168, AUX_SC1);
    ST(6)
    asm volatile("s_waitcnt vmcnt(0)":::"memory");
    ST(3)
  } else if (w >= 2 && b < 128) {
    const int tok = b & 7, cb = b >> 3;
    const int soff = P2_OFF + ((cb * 8 + tok) * 2 + (w - 2)) * 7168;
    v4u p[7];
    for (unsigned spin = 0; spin < SPIN_CAP; ++spin) {
      unsigned chk = 0;
#pragma unroll
      for (int i = 0; i < 7; ++i) p[i] = __builtin_amdgcn_raw_buffer_load_b128(rsS, l * 16, soff + i * 1024, AUX_SC1);
#pragma unroll
      for (int i = 0; i < 7; ++i) {
        p[i] = untag4(p[i], T);
        chk |= p[i].x | p[i].y | p[i].z | p[i].w;
      }
      if (__builtin_amdgcn_ballot_w64((chk & 0xFF) != 0) == 0) break;
      asm volatile("" ::: "memory");
    }
    ST(7)
    f32x4 s = __builtin_bit_cast(f32x4, p[0]);
#pragma unroll
    for (int i = 1; i < 7; ++i) s += __builtin_bit_cast(f32x4, p[i]);
    float res[4];
#pragma unroll
    for (int i = 0; i < 4; ++i) {
      float v = s[i];
      v += ror8(v);
      v += __shfl_xor(v, 16);
      v += __shfl_xor(v, 32);
      const float xv = (i & 1) ? asf(xres[i >> 1] & 0xFFFF0000u) : asf(xres[i >> 1] << 16);
      const float yy = rbf(rbf(v) * rsel);
      const float upd = rbf(rbf(xv * 0.0f) + yy);
      res[i] = xv + upd;
    }
    if (l < 8) {
      const __amdgpu_buffer_rsrc_t rsO = mkrs(out, 8 * D * 2);
      v2u ov = {pk_bf16(res[0], res[1]), pk_bf16(res[2], res[3])};
      __builtin_amdgcn_raw_buffer_store_b64(ov, rsO, nb * 16, tok * 2048 + cb * 128 + (w - 2) * 8, 0);
    }
    if (b == 0 && w == 2 && l == 0) __builtin_amdgcn_raw_buffer_store_b32(T, rsS, 0, 0, AUX_SC1);
  }
  {
    const __amdgpu_buffer_rsrc_t rsT = mkrs((char*)ws + STAMP_OFF, 1 << 20);
    unsigned mine = 0;
#pragma unroll
    for (int i = 0; i < 8; ++i) if (l == i) mine = (w >= 4 && i == 3) ? 0u : (unsigned)ts[i];
    if (l == 8) mine = (w >= 4) ? (unsigned)ts[3] : 0u;
    if (l < 16) __builtin_amdgcn_raw_buffer_store_b32(mine, rsT, l * 4, (b * 8 + w) * 64, AUX_SC1);
    if (b == 0 && w == 2) {
      unsigned t7 = (unsigned)ts[7];
      while ((unsigned)rt() - t7 < 1000u) __builtin_amdgcn_s_sleep(4);
      unsigned now = (unsigned)rt();
      unsigned mn0 = 0, sel_min = 0, sel_max = 0xFFFFFFFFu;
      for (int i = l; i < NWG * 8; i += 64) {
        unsigned a0 = now - __builtin_amdgcn_raw_buffer_load_b32(rsT, i * 64, 0, AUX_SC1);
        unsigned tj = __builtin_amdgcn_raw_buffer_load_b32(rsT, i * 64 + SJ * 4, 0, AUX_SC1);
        unsigned aj = now - tj;
        if (a0 < 6000u) mn0 = max(mn0, a0);
        if (tj != 0 && aj < 6000u) { sel_min = max(sel_min, aj); sel_max = min(sel_max, aj); }
      }
      for (int s = 1; s < 64; s <<= 1) {
        mn0 = max(mn0, (unsigned)__shfl_xor((int)mn0, s));
        sel_min = max(sel_min, (unsigned)__shfl_xor((int)sel_min, s));
        sel_max = min(sel_max, (unsigned)__shfl_xor((int)sel_max, s));
      }
      unsigned S = SMAX ? (mn0 - sel_max) : (mn0 - sel_min);
      if (S > 6000u) S = 6000u;
      unsigned start = now - mn0;
      unsigned target = __builtin_amdgcn_readfirstlane(5000u + SK * S);
      start = __builtin_amdgcn_readfirstlane(start);
      while ((unsigned)rt() - start < target) __builtin_amdgcn_s_sleep(1);
    }
  }
}

extern "C" void kernel_launch(void* const* d_in, const int* in_sizes, int n_in, void* d_out, int out_size, void* d_ws,
                              size_t ws_size, hipStream_t stream) {
  moe_decode<<<NWG, 512, 0, stream>>>(d_in[0], d_in[1], d_in[2], d_in[3], d_in[4], d_out, d_ws);
}
```

```cpp
#define SJ 0
#define SMAX 1
#define SK 0
#include <hip/hip_runtime.h>
#include <stdint.h>

typedef unsigned v4u __attribute__((ext_vector_type(4)));
typedef unsigned v2u __attribute__((ext_vector_type(2)));
typedef float f32x4 __attribute__((ext_vector_type(4)));
typedef __bf16 bf16x8 __attribute__((ext_vector_type(8)));
typedef __bf16 bf16x2 __attribute__((ext_vector_type(2)));
typedef float f32x2 __attribute__((ext_vector_type(2)));
#define LAS __attribute__((address_space(3)))

#define WAUX 2
constexpr int D = 1024, F = 3584;
constexpr int NWG = 224;
constexpr int AUX_SC1 = 16;
constexpr int P1_OFF = 4096, P2_OFF = P1_OFF + NWG * 2048, WS_BYTES = P2_OFF + 256 * 7168;
constexpr unsigned SPIN_CAP = 1u << 16;

__device__ __forceinline__ __amdgpu_buffer_rsrc_t mkrs(const void* p, int bytes) {
  return __builtin_amdgcn_make_buffer_rsrc((void*)p, 0, bytes, 0x00020000);
}
__device__ __forceinline__ float asf(unsigned u) { return __builtin_bit_cast(float, u); }
__device__ __forceinline__ unsigned asu(float f) { return __builtin_bit_cast(unsigned, f); }
__device__ __forceinline__ unsigned pk_bf16(float a, float b) {
  f32x2 v = {a, b};
  return __builtin_bit_cast(unsigned, __builtin_convertvector(v, bf16x2));
}
__device__ __forceinline__ float rbf(float a) { return asf(pk_bf16(a, a) << 16); }
__device__ __forceinline__ float ror8(float v) {
  return asf(__builtin_amdgcn_update_dpp(0u, asu(v), 0x128, 0xf, 0xf, true));
}
__device__ __forceinline__ void tile_mma(const v4u (&a)[8], v4u xf, bool hi, float (&o)[8]) {
#pragma unroll
  for (int cc = 0; cc < 8; ++cc) {
    const unsigned sel = (cc & 1) ? 0x07060302u : 0x05040100u;
    v4u b;
    b.x = __builtin_amdgcn_perm(a[1][cc >> 1], a[0][cc >> 1], sel);
    b.y = __builtin_amdgcn_perm(a[3][cc >> 1], a[2][cc >> 1], sel);
    b.z = __builtin_amdgcn_perm(a[5][cc >> 1], a[4][cc >> 1], sel);
    b.w = __builtin_amdgcn_perm(a[7][cc >> 1], a[6][cc >> 1], sel);
    f32x4 z = {0.f, 0.f, 0.f, 0.f};
    f32x4 d = __builtin_amdgcn_mfma_f32_16x16x32_bf16(__builtin_bit_cast(bf16x8, xf), __builtin_bit_cast(bf16x8, b), z, 0, 0, 0);
    float own = hi ? d[3] : d[0];
    float oth = hi ? d[1] : d[2];
    o[cc] = own + ror8(oth);
  }
}
__device__ __forceinline__ v4u tag4(f32x4 v, unsigned T) {
  v4u r;
  r.x = (asu(v[0]) & ~0xFFu) | (T & 0xFF);
  r.y = (asu(v[1]) & ~0xFFu) | ((T >> 8) & 0xFF);
  r.z = (asu(v[2]) & ~0xFFu) | ((T >> 16) & 0xFF);
  r.w = (asu(v[3]) & ~0xFFu) | (T >> 24);
  return r;
}
__device__ __forceinline__ v4u untag4(v4u v, unsigned T) {
  v4u r;
  r.x = v.x ^ (T & 0xFF);
  r.y = v.y ^ ((T >> 8) & 0xFF);
  r.z = v.z ^ ((T >> 16) & 0xFF);
  r.w = v.w ^ (T >> 24);
  return r;
}

#define STAMP_OFF (8<<20)
__device__ __forceinline__ unsigned long long rt(){ unsigned long long t; asm volatile("s_memrealtime %0\n\ts_waitcnt lgkmcnt(0)":"=s"(t)::"memory"); return t; }
#define ST(i) { __builtin_amdgcn_sched_barrier(0); ts[i]=rt(); __builtin_amdgcn_sched_barrier(0); }
__global__ void __launch_bounds__(512, 2) moe_decode(const void* x, const void* wr, const void* wg, const void* wu,
                                                     const void* wd, void* out, void* ws) {
  __shared__ __attribute__((aligned(16))) unsigned char smem[84 * 1024];
  unsigned long long ts[8]={0,0,0,0,0,0,0,0};
  ST(0)
  LAS unsigned char* L = (LAS unsigned char*)smem;
  const int l = threadIdx.x & 63, w = __builtin_amdgcn_readfirstlane(threadIdx.x >> 6);
  const int b = blockIdx.x;
  const int bb = (b & 7) * 28 + (b >> 3);
  const int grp = bb >> 2, m = bb & 3;
  const int q = l >> 4, r = (l >> 3) & 1, nb = l & 7;
  const bool hi = r != 0;

  const __amdgpu_buffer_rsrc_t rsW = mkrs((m & 1) ? wu : wg, D * F * 2);
  const __amdgpu_buffer_rsrc_t rsD = mkrs(wd, D * F * 2);
  const __amdgpu_buffer_rsrc_t rsX = mkrs(x, 8 * D * 2);
  const __amdgpu_buffer_rsrc_t rsS = mkrs(ws, WS_BYTES);

  const int k0 = 512 * (m >> 1) + 64 * w;
  v4u a[8];
  {
    const int voff = (32 * r + 8 * q) * (F * 2) + nb * 16;
    const int soff = k0 * (F * 2) + grp * 128;
#pragma unroll
    for (int j = 0; j < 8; ++j) a[j] = __builtin_amdgcn_raw_buffer_load_b128(rsW, voff, soff + j * (F * 2), WAUX);
  }
  const v4u xf = __builtin_amdgcn_raw_buffer_load_b128(rsX, ((l & 15) >> 1) * 2048 + (l & 1) * 64 + q * 16, k0 * 2, 0);
  const unsigned Ev = __builtin_amdgcn_raw_buffer_load_b32(rsS, 0, 0, AUX_SC1);
  v4u e[8];
  v4u xr0, xr1, wr0, wr1;
  v2u xres;
  if (w >= 4) {
    const int voff = (32 * r + 8 * q) * (D * 2) + nb * 16;
    const int soff = grp * 64 * (D * 2) + m * 512 + (w - 4) * 128;
#pragma unroll
    for (int j = 0; j < 8; ++j) e[j] = __builtin_amdgcn_raw_buffer_load_b128(rsD, voff, soff + j * (D * 2), WAUX);
  } else if (w >= 2 && b < 128) {
    const __amdgpu_buffer_rsrc_t rsR = mkrs(wr, D * 2);
    const int tok = b & 7, cb = b >> 3;
    xr0 = __builtin_amdgcn_raw_buffer_load_b128(rsX, l * 16, tok * 2048, 0);
    xr1 = __builtin_amdgcn_raw_buffer_load_b128(rsX, l * 16, tok * 2048 + 1024, 0);
    wr0 = __builtin_amdgcn_raw_buffer_load_b128(rsR, l * 16, 0, 0);
    wr1 = __builtin_amdgcn_raw_buffer_load_b128(rsR, l * 16, 1024, 0);
    xres = __builtin_amdgcn_raw_buffer_load_b64(rsX, nb * 16, tok * 2048 + cb * 128 + (w - 2) * 8, 0);
  }
  __builtin_amdgcn_sched_barrier(0);

  ST(1)
  float o[8];
  tile_mma(a, xf, hi, o);
  const unsigned T = __builtin_amdgcn_readfirstlane(Ev) + 0x01010101u;
  {
    LAS f32x4* red = (LAS f32x4*)L;
    red[w * 128 + l] = f32x4{o[0], o[1], o[2], o[3]};
    red[w * 128 + 64 + l] = f32x4{o[4], o[5], o[6], o[7]};
  }
  asm volatile("s_waitcnt lgkmcnt(0)" ::: "memory");
  __builtin_amdgcn_s_barrier();
  asm volatile("" ::: "memory");
  ST(2)
  float rsel = 0.f;
  if (w < 2) {
    LAS f32x4* red = (LAS f32x4*)L;
    f32x4 s = red[w * 64 + l];
#pragma unroll
    for (int i = 1; i < 8; ++i) s += red[i * 128 + w * 64 + l];
    __builtin_amdgcn_raw_buffer_store_b128(tag4(s, T), rsS, l * 16, P1_OFF + bb * 2048 + w * 1024, AUX_SC1);
    ST(3)
    v4u p[4];
    for (unsigned spin = 0; spin < SPIN_CAP; ++spin) {
      unsigned chk = 0;
#pragma unroll
      for (int i = 0; i < 4; ++i) p[i] = __builtin_amdgcn_raw_buffer_load_b128(rsS, l * 16, P1_OFF + (grp * 4 + i) * 2048 + w * 1024, AUX_SC1);
#pragma unroll
      for (int i = 0; i < 4; ++i) {
        p[i] = untag4(p[i], T);
        chk |= p[i].x | p[i].y | p[i].z | p[i].w;
      }
      if (__builtin_amdgcn_ballot_w64((chk & 0xFF) != 0) == 0) break;
      asm volatile("" ::: "memory");
    }
    ST(4)
    float h[4];
#pragma unroll
    for (int i = 0; i < 4; ++i) {
      const float g = rbf(asf(p[0][i]) + asf(p[2][i]));
      const float u = rbf(asf(p[1][i]) + asf(p[3][i]));
      const float s1 = rbf(g * __builtin_amdgcn_rcpf(1.0f + __expf(-g)));
      h[i] = s1 * u;
    }
    v2u hv = {pk_bf16(h[0], h[1]), pk_bf16(h[2], h[3])};
    *(LAS v2u*)(L + 16384 + l * 16 + w * 8) = hv;
  } else if (w < 4 && b < 128) {
    float acc = 0.f;
#pragma unroll
    for (int i = 0; i < 4; ++i) {
      acc = fmaf(asf(xr0[i] << 16), asf(wr0[i] << 16), acc);
      acc = fmaf(asf(xr0[i] & 0xFFFF0000u), asf(wr0[i] & 0xFFFF0000u), acc);
      acc = fmaf(asf(xr1[i] << 16), asf(wr1[i] << 16), acc);
      acc = fmaf(asf(xr1[i] & 0xFFFF0000u), asf(wr1[i] & 0xFFFF0000u), acc);
    }
#pragma unroll
    for (int s = 1; s < 64; s <<= 1) acc += __shfl_xor(acc, s);
    const float rr = rbf(acc);
    rsel = rbf(__builtin_amdgcn_rcpf(1.0f + __expf(-rr)));
  }
  asm volatile("s_waitcnt lgkmcnt(0)" ::: "memory");
  __builtin_amdgcn_s_barrier();
  asm volatile("" ::: "memory");
  ST(5)
  if (w >= 4) {
    const v4u hf = *(LAS v4u*)(L + 16384 + ((l & 15) >> 1) * 128 + ((l & 1) * 4 + q) * 16);
    float y[8];
    tile_mma(e, hf, hi, y);
    const int voff = (l >> 3) * (2 * 7168) + nb * 16;
    const int soff = P2_OFF + (m * 4 + (w - 4)) * (16 * 7168) + grp * 128;
    __builtin_amdgcn_raw_buffer_store_b128(tag4(f32x4{y[0], y[1], y[2], y[3]}, T), rsS, voff, soff, AUX_SC1);
    __builtin_amdgcn_raw_buffer_store_b128(tag4(f32x4{y[4], y[5], y[6], y[7]}, T), rsS, voff, soff + 7168, AUX_SC1);
    ST(6)
    asm volatile("s_waitcnt vmcnt(0)":::"memory");
    ST(3)
  } else if (w >= 2 && b < 128) {
    const int tok = b & 7, cb = b >> 3;
    const int soff = P2_OFF + ((cb * 8 + tok) * 2 + (w - 2)) * 7168;
    v4u p[7];
    for (unsigned spin = 0; spin < SPIN_CAP; ++spin) {
      unsigned chk = 0;
#pragma unroll
      for (int i = 0; i < 7; ++i) p[i] = __builtin_amdgcn_raw_buffer_load_b128(rsS, l * 16, soff + i * 1024, AUX_SC1);
#pragma unroll
      for (int i = 0; i < 7; ++i) {
        p[i] = untag4(p[i], T);
        chk |= p[i].x | p[i].y | p[i].z | p[i].w;
      }
      if (__builtin_amdgcn_ballot_w64((chk & 0xFF) != 0) == 0) break;
      asm volatile("" ::: "memory");
    }
    ST(7)
    f32x4 s = __builtin_bit_cast(f32x4, p[0]);
#pragma unroll
    for (int i = 1; i < 7; ++i) s += __builtin_bit_cast(f32x4, p[i]);
    float res[4];
#pragma unroll
    for (int i = 0; i < 4; ++i) {
      float v = s[i];
      v += ror8(v);
      v += __shfl_xor(v, 16);
      v += __shfl_xor(v, 32);
      const float xv = (i & 1) ? asf(xres[i >> 1] & 0xFFFF0000u) : asf(xres[i >> 1] << 16);
      const float yy = rbf(rbf(v) * rsel);
      const float upd = rbf(rbf(xv * 0.0f) + yy);
      res[i] = xv + upd;
    }
    if (l < 8) {
      const __amdgpu_buffer_rsrc_t rsO = mkrs(out, 8 * D * 2);
      v2u ov = {pk_bf16(res[0], res[1]), pk_bf16(res[2], res[3])};
      __builtin_amdgcn_raw_buffer_store_b64(ov, rsO, nb * 16, tok * 2048 + cb * 128 + (w - 2) * 8, 0);
    }
    if (b == 0 && w == 2 && l == 0) __builtin_amdgcn_raw_buffer_store_b32(T, rsS, 0, 0, AUX_SC1);
  }
  {
    const __amdgpu_buffer_rsrc_t rsT = mkrs((char*)ws + STAMP_OFF, 1 << 20);
    unsigned mine = 0;
#pragma unroll
    for (int i = 0; i < 8; ++i) if (l == i) mine = (w >= 4 && i == 3) ? 0u : (unsigned)ts[i];
    if (l == 8) mine = (w >= 4) ? (unsigned)ts[3] : 0u;
    if (l < 16) __builtin_amdgcn_raw_buffer_store_b32(mine, rsT, l * 4, (b * 8 + w) * 64, AUX_SC1);
    if (b == 0 && w == 2) {
      unsigned t7 = (unsigned)ts[7];
      while ((unsigned)rt() - t7 < 1000u) __builtin_amdgcn_s_sleep(4);
      unsigned now = (unsigned)rt();
      unsigned mn0 = 0, sel_min = 0, sel_max = 0xFFFFFFFFu;
      for (int i = l; i < NWG * 8 + 1; i += 64) {
        unsigned a0 = now - __builtin_amdgcn_raw_buffer_load_b32(rsT, i * 64, 0, AUX_SC1);
        unsigned tj = __builtin_amdgcn_raw_buffer_load_b32(rsT, i * 64 + SJ * 4, 0, AUX_SC1);
        unsigned aj = now - tj;
        if (a0 < 6000u) mn0 = max(mn0, a0);
        if (tj != 0 && aj < 6000u) { sel_min = max(sel_min, aj); sel_max = min(sel_max, aj); }
      }
      for (int s = 1; s < 64; s <<= 1) {
        mn0 = max(mn0, (unsigned)__shfl_xor((int)mn0, s));
        sel_min = max(sel_min, (unsigned)__shfl_xor((int)sel_min, s));
        sel_max = min(sel_max, (unsigned)__shfl_xor((int)sel_max, s));
      }
      unsigned S = SMAX ? (mn0 - sel_max) : (mn0 - sel_min);
      if (S > 6000u) S = 6000u;
      unsigned start = now - mn0;
      unsigned target = __builtin_amdgcn_readfirstlane(5000u + SK * S);
      start = __builtin_amdgcn_readfirstlane(start);
      while ((unsigned)rt() - start < target) __builtin_amdgcn_s_sleep(1);
    }
  }
}

__global__ void pre_kernel(void* ws){
  unsigned t=(unsigned)rt();
  const __amdgpu_buffer_rsrc_t rsT = mkrs((char*)ws + STAMP_OFF, 1 << 20);
  if(threadIdx.x==0) __builtin_amdgcn_raw_buffer_store_b32(t, rsT, 0, NWG*8*64, AUX_SC1);
}
extern "C" void kernel_launch(void* const* d_in, const int* in_sizes, int n_in, void* d_out, int out_size, void* d_ws,
                              size_t ws_size, hipStream_t stream) {
  pre_kernel<<<1,64,0,stream>>>(d_ws);
  moe_decode<<<NWG, 512, 0, stream>>>(d_in[0], d_in[1], d_in[2], d_in[3], d_in[4], d_out, d_ws);
}
```

```cpp
#define PROBE 4
#include <hip/hip_runtime.h>
#include <stdint.h>

typedef unsigned v4u __attribute__((ext_vector_type(4)));
typedef unsigned v2u __attribute__((ext_vector_type(2)));
typedef float f32x4 __attribute__((ext_vector_type(4)));
typedef __bf16 bf16x8 __attribute__((ext_vector_type(8)));
typedef __bf16 bf16x2 __attribute__((ext_vector_type(2)));
typedef float f32x2 __attribute__((ext_vector_type(2)));
#define LAS __attribute__((address_space(3)))

#define WAUX 2
constexpr int D = 1024, F = 3584;
constexpr int NWG = 224;
constexpr int AUX_SC1 = 16;
constexpr int P1_OFF = 4096, P2_OFF = P1_OFF + NWG * 2048, WS_BYTES = P2_OFF + 256 * 7168;
constexpr unsigned SPIN_CAP = 1u << 16;

__device__ __forceinline__ __amdgpu_buffer_rsrc_t mkrs(const void* p, int bytes) {
  return __builtin_amdgcn_make_buffer_rsrc((void*)p, 0, bytes, 0x00020000);
}
__device__ __forceinline__ float asf(unsigned u) { return __builtin_bit_cast(float, u); }
__device__ __forceinline__ unsigned asu(float f) { return __builtin_bit_cast(unsigned, f); }
__device__ __forceinline__ unsigned pk_bf16(float a, float b) {
  f32x2 v = {a, b};
  return __builtin_bit_cast(unsigned, __builtin_convertvector(v, bf16x2));
}
__device__ __forceinline__ float rbf(float a) { return asf(pk_bf16(a, a) << 16); }
__device__ __forceinline__ float ror8(float v) {
  return asf(__builtin_amdgcn_update_dpp(0u, asu(v), 0x128, 0xf, 0xf, true));
}
__device__ __forceinline__ void tile_mma(const v4u (&a)[8], v4u xf, bool hi, float (&o)[8]) {
#pragma unroll
  for (int cc = 0; cc < 8; ++cc) {
    const unsigned sel = (cc & 1) ? 0x07060302u : 0x05040100u;
    v4u b;
    b.x = __builtin_amdgcn_perm(a[1][cc >> 1], a[0][cc >> 1], sel);
    b.y = __builtin_amdgcn_perm(a[3][cc >> 1], a[2][cc >> 1], sel);
    b.z = __builtin_amdgcn_perm(a[5][cc >> 1], a[4][cc >> 1], sel);
    b.w = __builtin_amdgcn_perm(a[7][cc >> 1], a[6][cc >> 1], sel);
    f32x4 z = {0.f, 0.f, 0.f, 0.f};
    f32x4 d = __builtin_amdgcn_mfma_f32_16x16x32_bf16(__builtin_bit_cast(bf16x8, xf), __builtin_bit_cast(bf16x8, b), z, 0, 0, 0);
    float own = hi ? d[3] : d[0];
    float oth = hi ? d[1] : d[2];
    o[cc] = own + ror8(oth);
  }
}
__device__ __forceinline__ v4u tag4(f32x4 v, unsigned T) {
  v4u r;
  r.x = (asu(v[0]) & ~0xFFu) | (T & 0xFF);
  r.y = (asu(v[1]) & ~0xFFu) | ((T >> 8) & 0xFF);
  r.z = (asu(v[2]) & ~0xFFu) | ((T >> 16) & 0xFF);
  r.w = (asu(v[3]) & ~0xFFu) | (T >> 24);
  return r;
}
__device__ __forceinline__ v4u untag4(v4u v, unsigned T) {
  v4u r;
  r.x = v.x ^ (T & 0xFF);
  r.y = v.y ^ ((T >> 8) & 0xFF);
  r.z = v.z ^ ((T >> 16) & 0xFF);
  r.w = v.w ^ (T >> 24);
  return r;
}

__device__ __forceinline__ unsigned rt(){ unsigned long long t; asm volatile("s_memrealtime %0\n\ts_waitcnt lgkmcnt(0)":"=s"(t)::"memory"); return (unsigned)t; }
__device__ __forceinline__ unsigned ct(){ unsigned long long t; asm volatile("s_memtime %0\n\ts_waitcnt lgkmcnt(0)":"=s"(t)::"memory"); return (unsigned)t; }
__device__ __forceinline__ unsigned probe(const void* p, int off){
  const __amdgpu_buffer_rsrc_t rs = mkrs(p, 0x7000000);
  __builtin_amdgcn_sched_barrier(0);
  unsigned a=ct();
  __builtin_amdgcn_sched_barrier(0);
  unsigned v=__builtin_amdgcn_raw_buffer_load_b32(rs, 0, off, 0);
  asm volatile("s_waitcnt vmcnt(0)"::"v"(v):"memory");
  unsigned b=ct();
  __builtin_amdgcn_sched_barrier(0);
  return b-a;
}
__global__ void __launch_bounds__(512, 2) moe_decode(const void* x, const void* wr, const void* wg, const void* wu,
                                                     const void* wd, void* out, void* ws) {
  __shared__ __attribute__((aligned(16))) unsigned char smem[84 * 1024];
  unsigned tstart = rt();
  unsigned S = 0;
  if (blockIdx.x == 0 && __builtin_amdgcn_readfirstlane(threadIdx.x >> 6) == 2) {
    unsigned p[8];
    const char* ptrs[1];
    unsigned res0=0,res1=0,res2=0,res3=0,res4=0,res5=0;
    for (int it = 0; it < 7; ++it) {
      const void* q = it == 0 ? x : (it == 6 ? (const void*)ws : wg);
      int off = it == 2 ? 2048 : it == 3 ? 8192+256 : it == 4 ? (128 << 10)+512 : it == 5 ? (5 << 20)+768 : it == 6 ? 65536 : 0;
      unsigned d = probe(q, off);
      if (it == PROBE) S = d;
    }
  }
  while (rt() - tstart < 2000u) __builtin_amdgcn_s_sleep(8);
  LAS unsigned char* L = (LAS unsigned char*)smem;
  const int l = threadIdx.x & 63, w = __builtin_amdgcn_readfirstlane(threadIdx.x >> 6);
  const int b = blockIdx.x;
  const int bb = (b & 7) * 28 + (b >> 3);
  const int grp = bb >> 2, m = bb & 3;
  const int q = l >> 4, r = (l >> 3) & 1, nb = l & 7;
  const bool hi = r != 0;

  const __amdgpu_buffer_rsrc_t rsW = mkrs((m & 1) ? wu : wg, D * F * 2);
  const __amdgpu_buffer_rsrc_t rsD = mkrs(wd, D * F * 2);
  const __amdgpu_buffer_rsrc_t rsX = mkrs(x, 8 * D * 2);
  const __amdgpu_buffer_rsrc_t rsS = mkrs(ws, WS_BYTES);

  const int k0 = 512 * (m >> 1) + 64 * w;
  v4u a[8];
  {
    const int voff = (32 * r + 8 * q) * (F * 2) + nb * 16;
    const int soff = k0 * (F * 2) + grp * 128;
#pragma unroll
    for (int j = 0; j < 8; ++j) a[j] = __builtin_amdgcn_raw_buffer_load_b128(rsW, voff, soff + j * (F * 2), WAUX);
  }
  const v4u xf = __builtin_amdgcn_raw_buffer_load_b128(rsX, ((l & 15) >> 1) * 2048 + (l & 1) * 64 + q * 16, k0 * 2, 0);
  const unsigned Ev = __builtin_amdgcn_raw_buffer_load_b32(rsS, 0, 0, AUX_SC1);
  v4u e[8];
  v4u xr0, xr1, wr0, wr1;
  v2u xres;
  if (w >= 4) {
    const int voff = (32 * r + 8 * q) * (D * 2) + nb * 16;
    const int soff = grp * 64 * (D * 2) + m * 512 + (w - 4) * 128;
#pragma unroll
    for (int j = 0; j < 8; ++j) e[j] = __builtin_amdgcn_raw_buffer_load_b128(rsD, voff, soff + j * (D * 2), WAUX);
  } else if (w >= 2 && b < 128) {
    const __amdgpu_buffer_rsrc_t rsR = mkrs(wr, D * 2);
    const int tok = b & 7, cb = b >> 3;
    xr0 = __builtin_amdgcn_raw_buffer_load_b128(rsX, l * 16, tok * 2048, 0);
    xr1 = __builtin_amdgcn_raw_buffer_load_b128(rsX, l * 16, tok * 2048 + 1024, 0);
    wr0 = __builtin_amdgcn_raw_buffer_load_b128(rsR, l * 16, 0, 0);
    wr1 = __builtin_amdgcn_raw_buffer_load_b128(rsR, l * 16, 1024, 0);
    xres = __builtin_amdgcn_raw_buffer_load_b64(rsX, nb * 16, tok * 2048 + cb * 128 + (w - 2) * 8, 0);
  }
  __builtin_amdgcn_sched_barrier(0);

  float o[8];
  tile_mma(a, xf, hi, o);
  const unsigned T = __builtin_amdgcn_readfirstlane(Ev) + 0x01010101u;
  {
    LAS f32x4* red = (LAS f32x4*)L;
    red[w * 128 + l] = f32x4{o[0], o[1], o[2], o[3]};
    red[w * 128 + 64 + l] = f32x4{o[4], o[5], o[6], o[7]};
  }
  asm volatile("s_waitcnt lgkmcnt(0)" ::: "memory");
  __builtin_amdgcn_s_barrier();
  asm volatile("" ::: "memory");
  float rsel = 0.f;
  if (w < 2) {
    LAS f32x4* red = (LAS f32x4*)L;
    f32x4 s = red[w * 64 + l];
#pragma unroll
    for (int i = 1; i < 8; ++i) s += red[i * 128 + w * 64 + l];
    __builtin_amdgcn_raw_buffer_store_b128(tag4(s, T), rsS, l * 16, P1_OFF + bb * 2048 + w * 1024, AUX_SC1);
    v4u p[4];
    for (unsigned spin = 0; spin < SPIN_CAP; ++spin) {
      unsigned chk = 0;
#pragma unroll
      for (int i = 0; i < 4; ++i) p[i] = __builtin_amdgcn_raw_buffer_load_b128(rsS, l * 16, P1_OFF + (grp * 4 + i) * 2048 + w * 1024, AUX_SC1);
#pragma unroll
      for (int i = 0; i < 4; ++i) {
        p[i] = untag4(p[i], T);
        chk |= p[i].x | p[i].y | p[i].z | p[i].w;
      }
      if (__builtin_amdgcn_ballot_w64((chk & 0xFF) != 0) == 0) break;
      asm volatile("" ::: "memory");
    }
    float h[4];
#pragma unroll
    for (int i = 0; i < 4; ++i) {
      const float g = rbf(asf(p[0][i]) + asf(p[2][i]));
      const float u = rbf(asf(p[1][i]) + asf(p[3][i]));
      const float s1 = rbf(g * __builtin_amdgcn_rcpf(1.0f + __expf(-g)));
      h[i] = s1 * u;
    }
    v2u hv = {pk_bf16(h[0], h[1]), pk_bf16(h[2], h[3])};
    *(LAS v2u*)(L + 16384 + l * 16 + w * 8) = hv;
  } else if (w < 4 && b < 128) {
    float acc = 0.f;
#pragma unroll
    for (int i = 0; i < 4; ++i) {
      acc = fmaf(asf(xr0[i] << 16), asf(wr0[i] << 16), acc);
      acc = fmaf(asf(xr0[i] & 0xFFFF0000u), asf(wr0[i] & 0xFFFF0000u), acc);
      acc = fmaf(asf(xr1[i] << 16), asf(wr1[i] << 16), acc);
      acc = fmaf(asf(xr1[i] & 0xFFFF0000u), asf(wr1[i] & 0xFFFF0000u), acc);
    }
#pragma unroll
    for (int s = 1; s < 64; s <<= 1) acc += __shfl_xor(acc, s);
    const float rr = rbf(acc);
    rsel = rbf(__builtin_amdgcn_rcpf(1.0f + __expf(-rr)));
  }
  asm volatile("s_waitcnt lgkmcnt(0)" ::: "memory");
  __builtin_amdgcn_s_barrier();
  asm volatile("" ::: "memory");
  if (w >= 4) {
    const v4u hf = *(LAS v4u*)(L + 16384 + ((l & 15) >> 1) * 128 + ((l & 1) * 4 + q) * 16);
    float y[8];
    tile_mma(e, hf, hi, y);
    const int voff = (l >> 3) * (2 * 7168) + nb * 16;
    const int soff = P2_OFF + (m * 4 + (w - 4)) * (16 * 7168) + grp * 128;
    __builtin_amdgcn_raw_buffer_store_b128(tag4(f32x4{y[0], y[1], y[2], y[3]}, T), rsS, voff, soff, AUX_SC1);
    __builtin_amdgcn_raw_buffer_store_b128(tag4(f32x4{y[4], y[5], y[6], y[7]}, T), rsS, voff, soff + 7168, AUX_SC1);
  } else if (w >= 2 && b < 128) {
    const int tok = b & 7, cb = b >> 3;
    const int soff = P2_OFF + ((cb * 8 + tok) * 2 + (w - 2)) * 7168;
    v4u p[7];
    for (unsigned spin = 0; spin < SPIN_CAP; ++spin) {
      unsigned chk = 0;
#pragma unroll
      for (int i = 0; i < 7; ++i) p[i] = __builtin_amdgcn_raw_buffer_load_b128(rsS, l * 16, soff + i * 1024, AUX_SC1);
#pragma unroll
      for (int i = 0; i < 7; ++i) {
        p[i] = untag4(p[i], T);
        chk |= p[i].x | p[i].y | p[i].z | p[i].w;
      }
      if (__builtin_amdgcn_ballot_w64((chk & 0xFF) != 0) == 0) break;
      asm volatile("" ::: "memory");
    }
    f32x4 s = __builtin_bit_cast(f32x4, p[0]);
#pragma unroll
    for (int i = 1; i < 7; ++i) s += __builtin_bit_cast(f32x4, p[i]);
    float res[4];
#pragma unroll
    for (int i = 0; i < 4; ++i) {
      float v = s[i];
      v += ror8(v);
      v += __shfl_xor(v, 16);
      v += __shfl_xor(v, 32);
      const float xv = (i & 1) ? asf(xres[i >> 1] & 0xFFFF0000u) : asf(xres[i >> 1] << 16);
      const float yy = rbf(rbf(v) * rsel);
      const float upd = rbf(rbf(xv * 0.0f) + yy);
      res[i] = xv + upd;
    }
    if (l < 8) {
      const __amdgpu_buffer_rsrc_t rsO = mkrs(out, 8 * D * 2);
      v2u ov = {pk_bf16(res[0], res[1]), pk_bf16(res[2], res[3])};
      __builtin_amdgcn_raw_buffer_store_b64(ov, rsO, nb * 16, tok * 2048 + cb * 128 + (w - 2) * 8, 0);
    }
    if (b == 0 && w == 2 && l == 0) __builtin_amdgcn_raw_buffer_store_b32(T, rsS, 0, 0, AUX_SC1);
    if (b == 0 && w == 2) { if (S > 20000u) S = 20000u; while (rt() - tstart < 5000u + S) __builtin_amdgcn_s_sleep(1); }
  }
}

extern "C" void kernel_launch(void* const* d_in, const int* in_sizes, int n_in, void* d_out, int out_size, void* d_ws,
                              size_t ws_size, hipStream_t stream) {
  moe_decode<<<NWG, 512, 0, stream>>>(d_in[0], d_in[1], d_in[2], d_in[3], d_in[4], d_out, d_ws);
}
```

```cpp
#include <hip/hip_runtime.h>
#include <stdint.h>

__global__ void __launch_bounds__(512, 2) __attribute__((aligned(4096)))
moe_decode(const void* wg, uint64_t dg, const void* wu, uint64_t du, const void* x, uint64_t dx, void* ws, uint64_t ds,
           const void* wd, uint64_t dd, const void* wr, uint64_t dr, void* out, uint64_t dout) {
  __shared__ __attribute__((aligned(16))) unsigned char smem[18 * 1024];
  asm volatile(
      "s_load_dwordx8 s[24:31], %0, 0x20\n\t"
      "s_and_b32 s4, %1, 8\n\t"
      "s_lshl_b32 s4, s4, 1\n\t"
      "s_load_dwordx4 s[20:23], %0, s4\n\t"
      "v_readfirstlane_b32 s5, %2\n\t"
      "v_lshlrev_b32 v7, 4, %2\n\t"
      "v_and_b32 v122, 0x3f0, v7\n\t"
      "s_cmpk_lg_u32 s5, 0x1c0\n\t"
      "s_cbranch_scc1 L_nocpf_%=\n\t"
      "s_load_dwordx8 s[36:43], %0, 0x50\n\t"
      "s_getpc_b64 s[60:61]\n\t"
      "s_and_b32 s60, s60, 0xfffff000\n\t"
      "v_lshlrev_b32 v3, 3, v122\n\t"
      "global_load_dwordx2 v[126:127], v3, s[60:61] offset:60\n\t"
      "L_nocpf_%=:\n\t"
      "s_and_b32 s6, %1, 7\n\t"
      "s_lshr_b32 s7, %1, 5\n\t"
      "s_add_u32 s8, s6, 3\n\t"
      "s_and_b32 s8, s8, 7\n\t"
      "s_cmp_eq_u32 s8, 7\n\t"
      "s_cselect_b32 s8, 0, s8\n\t"
      "s_mov_b32 s52, 0xff00ff00\n\t"
      "s_cmp_eq_u32 s7, 7\n\t"
      "s_cselect_b32 s9, s8, s7\n\t"
      "s_cselect_b32 s52, 0x00ff00ff, s52\n\t"
      "s_cmp_eq_u32 s9, s8\n\t"
      "s_cselect_b32 s52, s52, 0\n\t"
      "s_mov_b32 s53, s52\n\t"
      "s_mul_i32 s11, s6, 7\n\t"
      "s_add_u32 s11, s11, s9\n\t"
      "s_and_b32 s12, %1, 16\n\t"
      "s_lshl_b32 s12, s12, 5\n\t"
      "s_add_u32 s12, s12, s5\n\t"
      "s_movk_i32 s15, 0x1c00\n\t"
      "s_mul_i32 s13, s12, s15\n\t"
      "s_lshl_b32 s14, s11, 7\n\t"
      "s_add_u32 s44, s13, s14\n\t"
      "v_lshrrev_b32 v2, 1, %2\n\t"
      "v_and_b32 v2, 24, v2\n\t"
      "v_lshlrev_b32 v3, 2, %2\n\t"
      "v_and_or_b32 v2, v3, 32, v2\n\t"
      "v_and_b32 v4, 0x70, v7\n\t"
      "v_mad_u32_u24 v1, v2, s15, v4\n\t"
      "v_cndmask_b32 v1, v1, 2.0, s[52:53]\n\t"
      "v_and_b32 v5, 14, %2\n\t"
      "v_lshlrev_b32 v5, 10, v5\n\t"
      "v_and_b32 v6, 1, %2\n\t"
      "v_lshl_or_b32 v5, v6, 6, v5\n\t"
      "v_and_or_b32 v5, %2, 48, v5\n\t"
      "s_lshl_b32 s13, s12, 1\n\t"
      "s_cmp_eq_u32 s9, 2\n\t"
      "s_cselect_b32 s12, s8, s9\n\t"
      "s_cmp_lg_u32 s52, 0\n\t"
      "s_cselect_b32 s12, 2, s12\n\t"
      "s_cselect_b32 s8, 12, 0\n\t"
      "s_sub_u32 s16, 6, s12\n\t"
      "s_mul_i32 s16, s16, 6\n\t"
      "s_add_u32 s8, s8, s16\n\t"
      "s_lshl_b32 s19, s6, 5\n\t"
      "s_lshr_b32 s18, %1, 3\n\t"
      "s_add_u32 s19, s19, s18\n\t"
      "v_mov_b32 v121, 0\n\t"
      "v_and_b32 v3, 0x7c, v3\n\t"
      "ds_write_b32 v3, v121 offset:17408\n\t"
      "s_cmp_lg_u32 s5, 0\n\t"
      "s_waitcnt lgkmcnt(0)\n\t"
      "s_memrealtime s[34:35]\n\t"
      "s_cbranch_scc1 L_warm_%=\n\t"
      "L_go_%=:\n\t"
      "buffer_load_dwordx4 v[8:11], v1, s[20:23], s44 offen nt\n\t"
      "s_add_u32 s44, s44, s15\n\t"
      "buffer_load_dwordx4 v[12:15], v1, s[20:23], s44 offen nt\n\t"
      "s_add_u32 s44, s44, s15\n\t"
      "buffer_load_dwordx4 v[16:19], v1, s[20:23], s44 offen nt\n\t"
      "s_add_u32 s44, s44, s15\n\t"
      "buffer_load_dwordx4 v[20:23], v1, s[20:23], s44 offen nt\n\t"
      "s_add_u32 s44, s44, s15\n\t"
      "buffer_load_dwordx4 v[24:27], v1, s[20:23], s44 offen nt\n\t"
      "s_add_u32 s44, s44, s15\n\t"
      "buffer_load_dwordx4 v[28:31], v1, s[20:23], s44 offen nt\n\t"
      "s_add_u32 s44, s44, s15\n\t"
      "buffer_load_dwordx4 v[32:35], v1, s[20:23], s44 offen nt\n\t"
      "s_add_u32 s44, s44, s15\n\t"
      "buffer_load_dwordx4 v[36:39], v1, s[20:23], s44 offen nt\n\t"
      "s_cmp_lg_u32 s5, 0\n\t"
      "s_cbranch_scc1 L_compute_%=\n\t"
      "L_warm_%=:\n\t"
      "buffer_load_dwordx4 v[112:115], v5, s[24:27], s13 offen\n\t"
      "buffer_load_dword v116, off, s[28:31], 0 sc1\n\t"
      "s_barrier\n\t"
      "s_cbranch_scc0 L_compute_%=\n\t"
      "s_cmpk_eq_u32 s5, 0x1c0\n\t"
      "s_cbranch_scc1 L_redpre_%=\n\t"
      "s_add_u32 s14, s5, s18\n\t"
      "s_cmpk_lg_u32 s14, 92\n\t"
      "s_cbranch_scc1 L_warmback_%=\n\t"
      "v_mul_u32_u24 v118, 24960, v6\n\t"
      "s_lshl_b32 s17, s6, 16\n\t"
      "s_add_u32 s45, s17, 2104320\n\t"
      "s_xor_b32 s17, s45, 0x40000\n\t"
      "buffer_load_dword v127, v118, s[28:31], s17 offen\n\t"
      "s_lshl_b32 s16, s6, 4\n\t"
      "s_waitcnt lgkmcnt(0)\n\t"
      "v_mov_b32 v126, s34\n\t"
      "buffer_store_dword v126, off, s[28:31], s16 offset:1024 sc1\n\t"
      "L_warmback_%=:\n\t"
      "s_cmpk_lg_u32 s5, 0x100\n\t"
      "s_cbranch_scc1 L_nord_%=\n\t"
      "s_movk_i32 s10, 72\n\t"
      "s_call_b64 s[48:49], L_wait_%=\n\t"
      "buffer_load_dword v126, v4, s[28:31], 0 offen offset:1024 sc1\n\t"
      "L_nord_%=:\n\t"
      "s_mul_i32 s10, s5, 384\n\t"
      "s_lshr_b32 s10, s10, 10\n\t"
      "s_mul_i32 s16, s12, 3\n\t"
      "s_add_u32 s10, s10, s16\n\t"
      "s_sub_u32 s10, s10, 13\n\t"
      "s_getpc_b64 s[48:49]\n\t"
      "L_pc_%=:\n\t"
      "s_sub_u32 s48, s48, L_pc_%=-L_go_%=\n\t"
      "s_subb_u32 s49, s49, 0\n\t"
      "s_branch L_wait_%=\n\t"
      "L_wait0_%=:\n\t"
      "s_cmp_gt_i32 s60, -9\n\t"
      "s_cbranch_scc1 L_wait_%=\n\t"
      "s_sleep 2\n\t"
      "L_wait_%=:\n\t"
      "s_memrealtime s[60:61]\n\t"
      "s_waitcnt lgkmcnt(0)\n\t"
      "s_sub_u32 s60, s60, s34\n\t"
      "s_sub_u32 s60, s60, s10\n\t"
      "s_cbranch_scc1 L_wait0_%=\n\t"
      "s_setpc_b64 s[48:49]\n\t"
      "L_compute_%=:\n\t"
      "s_load_dwordx2 s[56:57], %0, 0x40\n\t"
      "s_mov_b32 s16, 0x05040100\n\t"
      "s_mov_b32 s17, 0x07060302\n\t"
      "s_lshl_b32 s18, s5, 4\n\t"
      "v_add_u32 v3, s18, v7\n\t"
      "v_mov_b32 v117, 0xff\n\t"
      "s_lshl_b32 s55, s19, 11\n\t"
      "s_add_u32 s55, s55, 4096\n\t"
      "s_lshr_b32 s18, s5, 4\n\t"
      "v_mov_b32 v5, s18\n\t"
      "s_waitcnt vmcnt(6)\n\t"
      "v_perm_b32 v40, v12, v8, s16\n\t"
      "v_perm_b32 v44, v12, v8, s17\n\t"
      "v_perm_b32 v48, v13, v9, s16\n\t"
      "v_perm_b32 v52, v13, v9, s17\n\t"
      "v_perm_b32 v56, v14, v10, s16\n\t"
      "v_perm_b32 v60, v14, v10, s17\n\t"
      "v_perm_b32 v64, v15, v11, s16\n\t"
      "v_perm_b32 v68, v15, v11, s17\n\t"
      "s_waitcnt vmcnt(4)\n\t"
      "v_perm_b32 v41, v20, v16, s16\n\t"
      "v_perm_b32 v45, v20, v16, s17\n\t"
      "v_perm_b32 v49, v21, v17, s16\n\t"
      "v_perm_b32 v53, v21, v17, s17\n\t"
      "v_perm_b32 v57, v22, v18, s16\n\t"
      "v_perm_b32 v61, v22, v18, s17\n\t"
      "v_perm_b32 v65, v23, v19, s16\n\t"
      "v_perm_b32 v69, v23, v19, s17\n\t"
      "s_waitcnt vmcnt(2)\n\t"
      "v_perm_b32 v42, v28, v24, s16\n\t"
      "v_perm_b32 v46, v28, v24, s17\n\t"
      "v_perm_b32 v50, v29, v25, s16\n\t"
      "v_perm_b32 v54, v29, v25, s17\n\t"
      "v_perm_b32 v58, v30, v26, s16\n\t"
      "v_perm_b32 v62, v30, v26, s17\n\t"
      "v_perm_b32 v66, v31, v27, s16\n\t"
      "v_perm_b32 v70, v31, v27, s17\n\t"
      "s_waitcnt vmcnt(0)\n\t"
      "v_perm_b32 v43, v36, v32, s16\n\t"
      "v_perm_b32 v47, v36, v32, s17\n\t"
      "v_perm_b32 v51, v37, v33, s16\n\t"
      "v_mfma_f32_16x16x32_bf16 v[72:75], v[112:115], v[40:43], 0\n\t"
      "ds_read_b32 v16, v5 offset:17420\n\t"
      "ds_read_b128 v[8:11], v3\n\t"
      "ds_read_b128 v[12:15], v3 offset:1024\n\t"
      "ds_read_b32 v17, v121 offset:17408\n\t"
      "s_memrealtime s[60:61]\n\t"
      "v_perm_b32 v55, v37, v33, s17\n\t"
      "v_mfma_f32_16x16x32_bf16 v[76:79], v[112:115], v[44:47], 0\n\t"
      "v_perm_b32 v59, v38, v34, s16\n\t"
      "v_mfma_f32_16x16x32_bf16 v[80:83], v[112:115], v[48:51], 0\n\t"
      "v_perm_b32 v63, v38, v34, s17\n\t"
      "v_mfma_f32_16x16x32_bf16 v[84:87], v[112:115], v[52:55], 0\n\t"
      "v_perm_b32 v67, v39, v35, s16\n\t"
      "v_mfma_f32_16x16x32_bf16 v[88:91], v[112:115], v[56:59], 0\n\t"
      "v_perm_b32 v71, v39, v35, s17\n\t"
      "v_readfirstlane_b32 s54, v116\n\t"
      "v_mfma_f32_16x16x32_bf16 v[92:95], v[112:115], v[60:63], 0\n\t"
      "v_add_f32_dpp v104, v73, v72 row_ror:8 row_mask:0xf bank_mask:0x3\n\t"
      "v_add_f32_dpp v105, v77, v76 row_ror:8 row_mask:0xf bank_mask:0x3\n\t"
      "v_mfma_f32_16x16x32_bf16 v[96:99], v[112:115], v[64:67], 0\n\t"
      "v_add_f32_dpp v106, v81, v80 row_ror:8 row_mask:0xf bank_mask:0x3\n\t"
      "v_add_f32_dpp v104, v74, v75 row_ror:8 row_mask:0xf bank_mask:0xc\n\t"
      "v_mfma_f32_16x16x32_bf16 v[100:103], v[112:115], v[68:71], 0\n\t"
      "v_add_f32_dpp v107, v85, v84 row_ror:8 row_mask:0xf bank_mask:0x3\n\t"
      "v_add_f32_dpp v105, v78, v79 row_ror:8 row_mask:0xf bank_mask:0xc\n\t"
      "v_add_f32_dpp v108, v89, v88 row_ror:8 row_mask:0xf bank_mask:0x3\n\t"
      "v_add_f32_dpp v106, v82, v83 row_ror:8 row_mask:0xf bank_mask:0xc\n\t"
      "v_add_f32_dpp v109, v93, v92 row_ror:8 row_mask:0xf bank_mask:0x3\n\t"
      "v_add_f32_dpp v107, v86, v87 row_ror:8 row_mask:0xf bank_mask:0xc\n\t"
      "s_add_u32 s54, s54, 0x01010101\n\t"
      "s_lshr_b32 s50, s54, 8\n\t"
      "v_add_f32_dpp v110, v97, v96 row_ror:8 row_mask:0xf bank_mask:0x3\n\t"
      "v_add_f32_dpp v108, v90, v91 row_ror:8 row_mask:0xf bank_mask:0xc\n\t"
      "v_add_f32_dpp v111, v101, v100 row_ror:8 row_mask:0xf bank_mask:0x3\n\t"
      "v_add_f32_dpp v109, v94, v95 row_ror:8 row_mask:0xf bank_mask:0xc\n\t"
      "s_lshr_b32 s51, s54, 16\n\t"
      "s_lshr_b32 s59, s54, 24\n\t"
      "s_cmp_eq_u32 s5, 0\n\t"
      "s_waitcnt lgkmcnt(0)\n\t"
      "v_cmp_eq_u32 vcc, 0, v16\n\t"
      "v_add_f32_dpp v110, v98, v99 row_ror:8 row_mask:0xf bank_mask:0xc\n\t"
      "v_add_f32_dpp v111, v102, v103 row_ror:8 row_mask:0xf bank_mask:0xc\n\t"
      "s_cbranch_vccnz L_chain_%=\n\t"
      "L_chainok_%=:\n\t"
      "s_cmpk_lg_u32 s5, 0x1c0\n\t"
      "v_pk_add_f32 v[104:105], v[8:9], v[104:105]\n\t"
      "v_pk_add_f32 v[106:107], v[10:11], v[106:107]\n\t"
      "v_pk_add_f32 v[108:109], v[12:13], v[108:109]\n\t"
      "v_pk_add_f32 v[110:111], v[14:15], v[110:111]\n\t"
      "s_cbranch_scc1 L_not7_%=\n\t"
      "v_readfirstlane_b32 s34, v17\n\t"
      "s_sub_u32 s60, s60, s34\n\t"
      "s_cmpk_lt_u32 s60, 246\n\t"
      "s_cbranch_scc0 L_pub_%=\n\t"
      "s_movk_i32 s10, 254\n\t"
      "s_call_b64 s[48:49], L_wait_%=\n\t"
      "L_pub_%=:\n\t"
      "v_bfi_b32 v104, v117, s54, v104\n\t"
      "v_bfi_b32 v105, v117, s50, v105\n\t"
      "v_bfi_b32 v106, v117, s51, v106\n\t"
      "v_bfi_b32 v107, v117, s59, v107\n\t"
      "buffer_store_dwordx4 v[104:107], v122, s[28:31], s55 offen sc1\n\t"
      "v_bfi_b32 v108, v117, s54, v108\n\t"
      "v_bfi_b32 v109, v117, s50, v109\n\t"
      "v_bfi_b32 v110, v117, s51, v110\n\t"
      "v_bfi_b32 v111, v117, s59, v111\n\t"
      "buffer_store_dwordx4 v[108:111], v122, s[28:31], s55 offen offset:1024 sc1\n\t"
      "ds_write_b32 v5, v117 offset:17424\n\t"
      "s_branch L_late3_%=\n\t"
      "L_chain_%=:\n\t"
      "s_cbranch_scc1 L_not7_%=\n\t"
      "L_chain2_%=:\n\t"
      "ds_read_b32 v16, v5 offset:17420\n\t"
      "ds_read_b128 v[8:11], v3\n\t"
      "ds_read_b128 v[12:15], v3 offset:1024\n\t"
      "s_waitcnt lgkmcnt(0)\n\t"
      "v_cmp_eq_u32 vcc, 0, v16\n\t"
      "s_cbranch_vccnz L_chain2_%=\n\t"
      "s_branch L_chainok_%=\n\t"
      "L_not7_%=:\n\t"
      "ds_write_b128 v3, v[104:107] offset:2048\n\t"
      "ds_write_b128 v3, v[108:111] offset:3072\n\t"
      "ds_write_b32 v5, v117 offset:17424\n\t"
      "s_cmpk_lt_u32 s5, 0x140\n\t"
      "s_cbranch_scc1 L_first_%=\n\t"
      "L_late_%=:\n\t"
      "s_cmp_eq_u32 s7, 7\n\t"
      "s_cbranch_scc1 L_done_%=\n\t"
      "L_wp_%=:\n\t"
      "s_sleep 1\n\t"
      "ds_read_b32 v40, v121 offset:17452\n\t"
      "s_waitcnt lgkmcnt(0)\n\t"
      "v_cmp_eq_u32 vcc, 0, v40\n\t"
      "s_cbranch_vccnz L_wp_%=\n\t"
      "L_late3_%=:\n\t"
      "ds_read_b32 v6, v121 offset:17408\n\t"
      "s_and_b32 s56, s54, 0xff\n\t"
      "s_and_b32 s57, s50, 0xff\n\t"
      "s_and_b32 s58, s51, 0xff\n\t"
      "s_sub_u32 s56, 0, s56\n\t"
      "s_subb_u32 s57, 0, s57\n\t"
      "s_sub_u32 s58, 0, s58\n\t"
      "s_subb_u32 s59, 0, s59\n\t"
      "s_waitcnt lgkmcnt(0)\n\t"
      "v_readfirstlane_b32 s34, v6\n\t"
      "s_sleep 6\n\t"
      "s_sub_u32 s10, 322, s8\n\t"
      "s_max_u32 s10, s10, 294\n\t"
      "s_call_b64 s[48:49], L_wait_%=\n\t"
      "s_and_b32 s14, s6, 3\n\t"
      "s_lshl_b32 s14, s14, 7\n\t"
      "buffer_load_dword v119, off, s[28:31], s14 offset:2048 sc1\n\t"
      "s_cmp_eq_u32 s7, 7\n\t"
      "s_cbranch_scc1 L_red_%=\n\t"
      "s_lshl_b32 s60, s6, 16\n\t"
      "s_lshl_b32 s61, s5, 3\n\t"
      "s_add_u32 s60, s60, s61\n\t"
      "s_add_u32 s60, s60, 2048\n\t"
      "s_lshl_b32 s61, s9, 13\n\t"
      "s_add_u32 s61, s61, s60\n\t"
      "s_add_u32 s62, s61, 0x1000\n\t"
      "s_add_u32 s60, s60, 0xe000\n\t"
      "s_add_u32 s63, s60, 0x1000\n\t"
      "v_and_b32 v120, 0x1f0, v122\n\t"
      "v_and_b32 v5, 32, %2\n\t"
      "v_lshl_or_b32 v120, v5, 6, v120\n\t"
      "v_and_b32 v121, 0x180, v122\n\t"
      "v_or_b32 v121, v121, v5\n\t"
      "v_lshrrev_b32 v5, 3, v4\n\t"
      "s_and_b32 s46, s5, 64\n\t"
      "s_lshl_b32 s46, s46, 3\n\t"
      "s_and_b32 s47, s5, 128\n\t"
      "s_lshr_b32 s47, s47, 1\n\t"
      "s_or_b32 s46, s46, s47\n\t"
      "v_or3_b32 v121, v121, v5, s46\n\t"
      "s_mov_b32 s45, 1048576\n\t"
      "s_mov_b32 s46, 0xbfb8aa3b\n\t"
      "L_poll1_%=:\n\t"
      "buffer_load_dwordx4 v[56:59], v120, s[28:31], s61 offen sc1\n\t"
      "buffer_load_dwordx4 v[60:63], v120, s[28:31], s62 offen sc1\n\t"
      "v_mov_b32 v6, 0\n\t"
      "s_cmp_eq_u32 s52, 0\n\t"
      "s_cbranch_scc0 L_p1s_%=\n\t"
      "s_waitcnt vmcnt(1)\n\t"
      "v_lshl_add_u64 v[56:57], v[56:57], 0, s[56:57]\n\t"
      "v_lshl_add_u64 v[58:59], v[58:59], 0, s[58:59]\n\t"
      "s_waitcnt vmcnt(0)\n\t"
      "v_lshl_add_u64 v[60:61], v[60:61], 0, s[56:57]\n\t"
      "v_lshl_add_u64 v[62:63], v[62:63], 0, s[58:59]\n\t"
      "v_pk_add_f32 v[88:89], v[56:57], v[60:61]\n\t"
      "v_pk_add_f32 v[90:91], v[58:59], v[62:63]\n\t"
      "L_p1c_%=:\n\t"
      "v_or3_b32 v5, v56, v57, v58\n\t"
      "v_or3_b32 v6, v6, v59, v60\n\t"
      "v_permlane32_swap_b32 v88, v90\n\t"
      "v_permlane32_swap_b32 v89, v91\n\t"
      "v_cvt_pk_bf16_f32 v88, 0, v88\n\t"
      "v_cvt_pk_bf16_f32 v89, 0, v89\n\t"
      "v_pk_mul_f32 v[92:93], v[88:89], s[46:47] op_sel_hi:[1,0]\n\t"
      "v_exp_f32 v92, v92\n\t"
      "v_exp_f32 v93, v93\n\t"
      "v_cvt_pk_bf16_f32 v90, 0, v90\n\t"
      "v_pk_add_f32 v[92:93], v[92:93], 1.0 op_sel_hi:[1,0]\n\t"
      "v_rcp_f32 v92, v92\n\t"
      "v_rcp_f32 v93, v93\n\t"
      "v_cvt_pk_bf16_f32 v91, 0, v91\n\t"
      "v_pk_mul_f32 v[92:93], v[88:89], v[92:93]\n\t"
      "v_cvt_pk_bf16_f32 v92, 0, v92\n\t"
      "v_cvt_pk_bf16_f32 v93, 0, v93\n\t"
      "v_pk_mul_f32 v[92:93], v[92:93], v[90:91]\n\t"
      "v_cvt_pk_bf16_f32 v92, v92, v93\n\t"
      "ds_write_b16 v121, v92 offset:16384\n\t"
      "ds_write_b16_d16_hi v121, v92 offset:16400\n\t"
      "v_or3_b32 v5, v5, v61, v62\n\t"
      "v_or3_b32 v5, v5, v6, v63\n\t"
      "v_and_b32 v5, v5, v117\n\t"
      "v_cmp_ne_u32 vcc, 0, v5\n\t"
      "s_cbranch_vccnz L_p1r_%=\n\t"
      "L_p1d_%=:\n\t"
      "s_waitcnt lgkmcnt(0)\n\t"
      "s_barrier\n\t"
      "s_cmpk_lg_u32 s5, 0x1c0\n\t"
      "s_cbranch_scc0 L_red_%=\n\t"
      "L_done_%=:\n\t"
      "s_endpgm\n\t"
      "L_p1r_%=:\n\t"
      "s_sub_u32 s45, s45, 1\n\t"
      "s_cbranch_scc0 L_poll1_%=\n\t"
      "s_branch L_p1d_%=\n\t"
      "L_p1s_%=:\n\t"
      "buffer_load_dwordx4 v[64:67], v120, s[28:31], s60 offen sc1\n\t"
      "buffer_load_dwordx4 v[68:71], v120, s[28:31], s63 offen sc1\n\t"
      "s_waitcnt vmcnt(2)\n\t"
      "v_lshl_add_u64 v[56:57], v[56:57], 0, s[56:57]\n\t"
      "v_lshl_add_u64 v[58:59], v[58:59], 0, s[58:59]\n\t"
      "v_lshl_add_u64 v[60:61], v[60:61], 0, s[56:57]\n\t"
      "v_lshl_add_u64 v[62:63], v[62:63], 0, s[58:59]\n\t"
      "v_pk_add_f32 v[88:89], v[56:57], v[60:61]\n\t"
      "v_pk_add_f32 v[90:91], v[58:59], v[62:63]\n\t"
      "s_waitcnt vmcnt(1)\n\t"
      "v_lshl_add_u64 v[64:65], v[64:65], 0, s[56:57]\n\t"
      "v_lshl_add_u64 v[66:67], v[66:67], 0, s[58:59]\n\t"
      "v_pk_add_f32 v[88:89], v[88:89], v[64:65]\n\t"
      "v_pk_add_f32 v[90:91], v[90:91], v[66:67]\n\t"
      "v_or3_b32 v6, v64, v65, v66\n\t"
      "s_waitcnt vmcnt(0)\n\t"
      "v_lshl_add_u64 v[68:69], v[68:69], 0, s[56:57]\n\t"
      "v_lshl_add_u64 v[70:71], v[70:71], 0, s[58:59]\n\t"
      "v_pk_add_f32 v[88:89], v[88:89], v[68:69]\n\t"
      "v_pk_add_f32 v[90:91], v[90:91], v[70:71]\n\t"
      "v_or3_b32 v6, v6, v67, v68\n\t"
      "v_or3_b32 v6, v6, v69, v70\n\t"
      "v_or_b32 v6, v6, v71\n\t"
      "s_branch L_p1c_%=\n\t"
      "L_first_%=:\n\t"
      "s_cmpk_lt_u32 s5, 0x100\n\t"
      "s_cbranch_scc1 L_early_%=\n\t"
      "s_sub_u32 s18, s34, 64\n\t"
      "v_subrev_u32 v126, s18, v126\n\t"
      "v_cmp_gt_u32 vcc, 0x80, v126\n\t"
      "s_nop 1\n\t"
      "v_cndmask_b32 v126, 64, v126, vcc\n\t"
      "v_mov_b32 v127, v126\n\t"
      "s_nop 0\n\t"
      "v_add_u32_dpp v126, v126, v126 row_ror:1 row_mask:0xf bank_mask:0xf\n\t"
      "v_max_u32_dpp v127, v127, v127 row_ror:1 row_mask:0xf bank_mask:0xf\n\t"
      "s_nop 0\n\t"
      "v_add_u32_dpp v126, v126, v126 row_ror:2 row_mask:0xf bank_mask:0xf\n\t"
      "v_max_u32_dpp v127, v127, v127 row_ror:2 row_mask:0xf bank_mask:0xf\n\t"
      "s_nop 0\n\t"
      "v_add_u32_dpp v126, v126, v126 row_ror:4 row_mask:0xf bank_mask:0xf\n\t"
      "v_max_u32_dpp v127, v127, v127 row_ror:4 row_mask:0xf bank_mask:0xf\n\t"
      "v_lshrrev_b32 v126, 3, v126\n\t"
      "v_subrev_u32 v127, 16, v127\n\t"
      "v_max_i32 v126, v126, v127\n\t"
      "v_add_u32 v126, s18, v126\n\t"
      "v_or_b32 v126, 1, v126\n\t"
      "ds_write_b32 v121, v126 offset:17408\n\t"
      "s_branch L_late_%=\n\t"
      "L_early_%=:\n\t"
      "s_cmp_eq_u32 s7, 7\n\t"
      "s_waitcnt lgkmcnt(0)\n\t"
      "s_cbranch_scc1 L_probe_%=\n\t"
      "s_mov_b64 s[20:21], s[56:57]\n\t"
      "v_lshrrev_b32 v120, 7, v122\n\t"
      "v_lshlrev_b32 v120, 15, v120\n\t"
      "v_add_u32 v120, v120, v4\n\t"
      "s_and_b32 s60, s19, 3\n\t"
      "s_lshl_b32 s61, s60, 8\n\t"
      "s_add_u32 s61, s61, s5\n\t"
      "s_lshr_b32 s62, s61, 7\n\t"
      "s_lshl_b32 s62, s62, 18\n\t"
      "s_and_b32 s63, s61, 64\n\t"
      "s_lshl_b32 s63, s63, 7\n\t"
      "s_add_u32 s62, s62, s63\n\t"
      "s_lshl3_add_u32 s60, s12, s6\n\t"
      "s_lshl_b32 s60, s60, 7\n\t"
      "s_add_u32 s62, s62, s60\n\t"
      "s_add_u32 s62, s62, 2097152\n\t"
      "s_add_u32 s63, s62, 0x4000\n\t"
      "v_lshl_or_b32 v1, v2, 8, v4\n\t"
      "s_lshl_b32 s44, s11, 17\n\t"
      "s_lshl_b32 s61, s61, 1\n\t"
      "s_add_u32 s44, s44, s61\n\t"
      "v_and_b32 v5, 15, %2\n\t"
      "v_lshlrev_b32 v5, 6, v5\n\t"
      "v_and_or_b32 v5, %2, 48, v5\n\t"
      "s_movk_i32 s10, 195\n\t"
      "s_call_b64 s[48:49], L_wait_%=\n\t"
      "buffer_load_dwordx4 v[8:11], v1, s[20:23], s44 offen nt\n\t"
      "s_addk_i32 s44, 0x4000\n\t"
      "L_wdl_%=:\n\t"
      "ds_read_b32 v6, v121 offset:17408\n\t"
      "s_waitcnt lgkmcnt(0)\n\t"
      "v_readfirstlane_b32 s34, v6\n\t"
      "s_cmp_eq_u32 s34, 0\n\t"
      "s_cbranch_scc1 L_wdl_%=\n\t"
      "s_sub_u32 s34, s34, s8\n\t"
      "s_bfe_u32 s61, s44, 0x30007\n\t"
      "s_cmp_eq_u32 s61, 3\n\t"
      "s_cselect_b32 s61, 24, 0\n\t"
      "s_sub_u32 s34, s34, s61\n\t"
      "s_movk_i32 s10, 255\n\t"
      "s_call_b64 s[48:49], L_wait_%=\n\t"
      "buffer_load_dwordx4 v[12:15], v1, s[20:23], s44 offen nt\n\t"
      "s_addk_i32 s44, 0x4000\n\t"
      "s_sleep 4\n\t"
      "buffer_load_dwordx4 v[16:19], v1, s[20:23], s44 offen nt\n\t"
      "s_addk_i32 s44, 0x4000\n\t"
      "s_sleep 3\n\t"
      "buffer_load_dwordx4 v[20:23], v1, s[20:23], s44 offen nt\n\t"
      "s_addk_i32 s44, 0x4000\n\t"
      "s_sleep 3\n\t"
      "buffer_load_dwordx4 v[24:27], v1, s[20:23], s44 offen nt\n\t"
      "s_addk_i32 s44, 0x4000\n\t"
      "s_sleep 3\n\t"
      "buffer_load_dwordx4 v[28:31], v1, s[20:23], s44 offen nt\n\t"
      "s_addk_i32 s44, 0x4000\n\t"
      "s_sleep 3\n\t"
      "buffer_load_dwordx4 v[32:35], v1, s[20:23], s44 offen nt\n\t"
      "s_addk_i32 s44, 0x4000\n\t"
      "s_sleep 3\n\t"
      "buffer_load_dwordx4 v[36:39], v1, s[20:23], s44 offen nt\n\t"
      "s_waitcnt vmcnt(6)\n\t"
      "v_perm_b32 v40, v12, v8, s16\n\t"
      "v_perm_b32 v44, v12, v8, s17\n\t"
      "v_perm_b32 v48, v13, v9, s16\n\t"
      "v_perm_b32 v52, v13, v9, s17\n\t"
      "v_perm_b32 v56, v14, v10, s16\n\t"
      "v_perm_b32 v60, v14, v10, s17\n\t"
      "v_perm_b32 v64, v15, v11, s16\n\t"
      "v_perm_b32 v68, v15, v11, s17\n\t"
      "s_waitcnt vmcnt(4)\n\t"
      "v_perm_b32 v41, v20, v16, s16\n\t"
      "v_perm_b32 v45, v20, v16, s17\n\t"
      "v_perm_b32 v49, v21, v17, s16\n\t"
      "v_perm_b32 v53, v21, v17, s17\n\t"
      "v_perm_b32 v57, v22, v18, s16\n\t"
      "v_perm_b32 v61, v22, v18, s17\n\t"
      "v_perm_b32 v65, v23, v19, s16\n\t"
      "v_perm_b32 v69, v23, v19, s17\n\t"
      "s_waitcnt vmcnt(2)\n\t"
      "v_perm_b32 v42, v28, v24, s16\n\t"
      "v_perm_b32 v46, v28, v24, s17\n\t"
      "v_perm_b32 v50, v29, v25, s16\n\t"
      "v_perm_b32 v54, v29, v25, s17\n\t"
      "v_perm_b32 v58, v30, v26, s16\n\t"
      "v_perm_b32 v62, v30, v26, s17\n\t"
      "v_perm_b32 v66, v31, v27, s16\n\t"
      "v_perm_b32 v70, v31, v27, s17\n\t"
      "s_waitcnt vmcnt(0)\n\t"
      "v_perm_b32 v43, v36, v32, s16\n\t"
      "v_perm_b32 v47, v36, v32, s17\n\t"
      "v_perm_b32 v51, v37, v33, s16\n\t"
      "v_perm_b32 v55, v37, v33, s17\n\t"
      "v_perm_b32 v59, v38, v34, s16\n\t"
      "v_perm_b32 v63, v38, v34, s17\n\t"
      "v_perm_b32 v67, v39, v35, s16\n\t"
      "v_perm_b32 v71, v39, v35, s17\n\t"
      "s_barrier\n\t"
      "ds_read_b128 v[112:115], v5 offset:16384\n\t"
      "s_waitcnt lgkmcnt(0)\n\t"
      "v_mfma_f32_16x16x32_bf16 v[72:75], v[112:115], v[40:43], 0\n\t"
      "v_mfma_f32_16x16x32_bf16 v[76:79], v[112:115], v[44:47], 0\n\t"
      "s_nop 1\n\t"
      "v_mfma_f32_16x16x32_bf16 v[80:83], v[112:115], v[48:51], 0\n\t"
      "s_nop 1\n\t"
      "v_mfma_f32_16x16x32_bf16 v[84:87], v[112:115], v[52:55], 0\n\t"
      "s_nop 0\n\t"
      "v_add_f32_dpp v104, v73, v72 row_ror:8 row_mask:0xf bank_mask:0x3\n\t"
      "v_mfma_f32_16x16x32_bf16 v[88:91], v[112:115], v[56:59], 0\n\t"
      "v_add_f32_dpp v105, v77, v76 row_ror:8 row_mask:0xf bank_mask:0x3\n\t"
      "v_add_f32_dpp v104, v74, v75 row_ror:8 row_mask:0xf bank_mask:0xc\n\t"
      "v_mfma_f32_16x16x32_bf16 v[92:95], v[112:115], v[60:63], 0\n\t"
      "v_add_f32_dpp v106, v81, v80 row_ror:8 row_mask:0xf bank_mask:0x3\n\t"
      "v_add_f32_dpp v105, v78, v79 row_ror:8 row_mask:0xf bank_mask:0xc\n\t"
      "v_mfma_f32_16x16x32_bf16 v[96:99], v[112:115], v[64:67], 0\n\t"
      "v_add_f32_dpp v107, v85, v84 row_ror:8 row_mask:0xf bank_mask:0x3\n\t"
      "v_add_f32_dpp v106, v82, v83 row_ror:8 row_mask:0xf bank_mask:0xc\n\t"
      "v_mfma_f32_16x16x32_bf16 v[100:103], v[112:115], v[68:71], 0\n\t"
      "v_add_f32_dpp v107, v86, v87 row_ror:8 row_mask:0xf bank_mask:0xc\n\t"
      "v_add_f32_dpp v108, v89, v88 row_ror:8 row_mask:0xf bank_mask:0x3\n\t"
      "v_bfi_b32 v104, v117, s54, v104\n\t"
      "v_bfi_b32 v105, v117, s50, v105\n\t"
      "v_bfi_b32 v106, v117, s51, v106\n\t"
      "v_bfi_b32 v107, v117, s59, v107\n\t"
      "buffer_store_dwordx4 v[104:107], v120, s[28:31], s62 offen sc1\n\t"
      "v_add_f32_dpp v109, v93, v92 row_ror:8 row_mask:0xf bank_mask:0x3\n\t"
      "v_add_f32_dpp v110, v97, v96 row_ror:8 row_mask:0xf bank_mask:0x3\n\t"
      "v_add_f32_dpp v111, v101, v100 row_ror:8 row_mask:0xf bank_mask:0x3\n\t"
      "v_add_f32_dpp v108, v90, v91 row_ror:8 row_mask:0xf bank_mask:0xc\n\t"
      "v_add_f32_dpp v109, v94, v95 row_ror:8 row_mask:0xf bank_mask:0xc\n\t"
      "v_add_f32_dpp v110, v98, v99 row_ror:8 row_mask:0xf bank_mask:0xc\n\t"
      "v_add_f32_dpp v111, v102, v103 row_ror:8 row_mask:0xf bank_mask:0xc\n\t"
      "v_bfi_b32 v108, v117, s54, v108\n\t"
      "v_bfi_b32 v109, v117, s50, v109\n\t"
      "v_bfi_b32 v110, v117, s51, v110\n\t"
      "v_bfi_b32 v111, v117, s59, v111\n\t"
      "buffer_store_dwordx4 v[108:111], v120, s[28:31], s63 offen sc1\n\t"
      "s_endpgm\n\t"
      "L_red_%=:\n\t"
      "s_mov_b32 s46, 1048576\n\t"
      "L_red1_%=:\n\t"
      "s_waitcnt vmcnt(0)\n\t"
      "v_readfirstlane_b32 s16, v119\n\t"
      "s_xor_b32 s17, s16, s54\n\t"
      "s_cmp_gt_u32 s17, 3\n\t"
      "s_cbranch_scc1 L_sdr_%=\n\t"
      "L_red2_%=:\n\t"
      "s_and_b32 s17, s16, s19\n\t"
      "s_lshr_b32 s17, s17, 1\n\t"
      "s_xor_b32 s16, s16, s17\n\t"
      "s_lshr_b32 s17, s6, 2\n\t"
      "s_xor_b32 s16, s16, s17\n\t"
      "s_and_b32 s16, s16, 1\n\t"
      "s_cbranch_scc0 L_red3_%=\n\t"
      "v_mov_b32 v124, v118\n\t"
      "s_addk_i32 s4, 0x80\n\t"
      "L_red3_%=:\n\t"
      "v_lshlrev_b32 v124, 16, v124\n\t"
      "s_and_b32 s45, s19, 0x7f\n\t"
      "v_mul_f32 v126, 0, v124\n\t"
      "s_lshl1_add_u32 s45, s45, s16\n\t"
      "s_lshl_b32 s45, s45, 13\n\t"
      "s_add_u32 s45, s45, 2097152\n\t"
      "s_add_u32 s47, s45, 0x1000\n\t"
      "s_getreg_b32 s16, hwreg(HW_REG_XCC_ID, 0, 3)\n\t"
      "s_sub_u32 s17, s16, s6\n\t"
      "v_bfe_u32 v12, v122, 7, 2\n\t"
      "v_add_u32 v12, s17, v12\n\t"
      "v_xor_b32 v12, s16, v12\n\t"
      "v_and_b32 v12, 4, v12\n\t"
      "v_lshlrev_b32 v12, 7, v12\n\t"
      "v_and_b32 v13, 0x1f0, v122\n\t"
      "v_or_b32 v12, v12, v13\n\t"
      "v_and_b32 v13, 0x200, v122\n\t"
      "v_lshl_or_b32 v12, v13, 1, v12\n\t"
      "v_xor_b32 v13, 0x200, v12\n\t"
      "L_rw_%=:\n\t"
      "s_memrealtime s[60:61]\n\t"
      "s_waitcnt lgkmcnt(0)\n\t"
      "s_sub_u32 s60, s60, s34\n\t"
      "s_cmpk_lt_u32 s60, 423\n\t"
      "s_cbranch_scc1 L_rw_%=\n\t"
      "buffer_load_dwordx4 v[28:31], v12, s[28:31], s45 offen offset:0 sc1\n\t"
      "s_sleep 1\n\t"
      "buffer_load_dwordx4 v[32:35], v12, s[28:31], s45 offen offset:2048 sc1\n\t"
      "s_sleep 1\n\t"
      "buffer_load_dwordx4 v[40:43], v13, s[28:31], s45 offen offset:0 sc1\n\t"
      "s_sleep 1\n\t"
      "buffer_load_dwordx4 v[36:39], v12, s[28:31], s47 offen offset:0 sc1\n\t"
      "s_sleep 1\n\t"
      "buffer_load_dwordx4 v[44:47], v13, s[28:31], s45 offen offset:2048 sc1\n\t"
      "s_sleep 1\n\t"
      "buffer_load_dwordx4 v[48:51], v13, s[28:31], s47 offen offset:0 sc1\n\t"
      "s_sleep 1\n\t"
      "buffer_load_dwordx4 v[52:55], v122, s[28:31], s47 offen offset:2048 sc1\n\t"
      "L_chk2_%=:\n\t"
      "s_waitcnt vmcnt(6)\n\t"
      "v_lshl_add_u64 v[28:29], v[28:29], 0, s[56:57]\n\t"
      "v_lshl_add_u64 v[30:31], v[30:31], 0, s[58:59]\n\t"
      "s_waitcnt vmcnt(5)\n\t"
      "v_lshl_add_u64 v[32:33], v[32:33], 0, s[56:57]\n\t"
      "v_lshl_add_u64 v[34:35], v[34:35], 0, s[58:59]\n\t"
      "s_waitcnt vmcnt(4)\n\t"
      "v_lshl_add_u64 v[40:41], v[40:41], 0, s[56:57]\n\t"
      "v_lshl_add_u64 v[42:43], v[42:43], 0, s[58:59]\n\t"
      "v_pk_add_f32 v[8:9], v[28:29], v[40:41]\n\t"
      "v_pk_add_f32 v[10:11], v[30:31], v[42:43]\n\t"
      "s_waitcnt vmcnt(3)\n\t"
      "v_lshl_add_u64 v[36:37], v[36:37], 0, s[56:57]\n\t"
      "v_lshl_add_u64 v[38:39], v[38:39], 0, s[58:59]\n\t"
      "s_waitcnt vmcnt(2)\n\t"
      "v_lshl_add_u64 v[44:45], v[44:45], 0, s[56:57]\n\t"
      "v_lshl_add_u64 v[46:47], v[46:47], 0, s[58:59]\n\t"
      "v_pk_add_f32 v[14:15], v[32:33], v[44:45]\n\t"
      "v_pk_add_f32 v[16:17], v[34:35], v[46:47]\n\t"
      "v_pk_add_f32 v[8:9], v[8:9], v[14:15]\n\t"
      "v_pk_add_f32 v[10:11], v[10:11], v[16:17]\n\t"
      "s_waitcnt vmcnt(1)\n\t"
      "v_lshl_add_u64 v[48:49], v[48:49], 0, s[56:57]\n\t"
      "v_lshl_add_u64 v[50:51], v[50:51], 0, s[58:59]\n\t"
      "v_pk_add_f32 v[14:15], v[36:37], v[48:49]\n\t"
      "v_pk_add_f32 v[16:17], v[38:39], v[50:51]\n\t"
      "v_pk_add_f32 v[8:9], v[8:9], v[14:15]\n\t"
      "v_pk_add_f32 v[10:11], v[10:11], v[16:17]\n\t"
      "s_waitcnt vmcnt(0)\n\t"
      "v_lshl_add_u64 v[52:53], v[52:53], 0, s[56:57]\n\t"
      "v_lshl_add_u64 v[54:55], v[54:55], 0, s[58:59]\n\t"
      "v_pk_add_f32 v[8:9], v[8:9], v[52:53]\n\t"
      "v_pk_add_f32 v[10:11], v[10:11], v[54:55]\n\t"
      "v_or3_b32 v5, v28, v29, v30\n\t"
      "v_permlane32_swap_b32 v8, v9\n\t"
      "v_permlane32_swap_b32 v10, v11\n\t"
      "v_add_f32 v8, v8, v9\n\t"
      "v_add_f32 v10, v10, v11\n\t"
      "v_or3_b32 v5, v5, v31, v32\n\t"
      "v_or3_b32 v5, v5, v33, v34\n\t"
      "v_permlane16_swap_b32 v8, v10\n\t"
      "v_add_f32 v8, v8, v10\n\t"
      "v_or3_b32 v5, v5, v35, v36\n\t"
      "v_or3_b32 v5, v5, v37, v38\n\t"
      "v_add_f32_dpp v8, v8, v8 row_ror:8 row_mask:0xf bank_mask:0xf\n\t"
      "v_cvt_pk_bf16_f32 v8, 0, v8\n\t"
      "v_mul_f32 v8, v8, v125\n\t"
      "v_cvt_pk_bf16_f32 v8, 0, v8\n\t"
      "v_add_f32 v8, v126, v8\n\t"
      "v_add_f32 v8, v124, v8\n\t"
      "v_cvt_pk_bf16_f32 v8, v8, v8\n\t"
      "buffer_store_short v8, v123, s[40:43], s4 offen\n\t"
      "v_or3_b32 v5, v5, v39, v40\n\t"
      "v_or3_b32 v5, v5, v41, v42\n\t"
      "v_or3_b32 v5, v5, v43, v44\n\t"
      "v_or3_b32 v5, v5, v45, v46\n\t"
      "v_or3_b32 v5, v5, v47, v48\n\t"
      "v_or3_b32 v5, v5, v49, v50\n\t"
      "v_or3_b32 v5, v5, v51, v52\n\t"
      "v_or3_b32 v5, v5, v53, v54\n\t"
      "v_bitop3_b32 v5, v5, v55, v117 bitop3:0xa8\n\t"
      "v_cmp_ne_u32 vcc, 0, v5\n\t"
      "s_cbranch_vccnz L_p2r_%=\n\t"
      "L_p2d_%=:\n\t"
      "s_endpgm\n\t"
      "L_p2r_%=:\n\t"
      "s_sub_u32 s46, s46, 1\n\t"
      "s_cbranch_scc1 L_p2d_%=\n\t"
      "buffer_load_dwordx4 v[28:31], v12, s[28:31], s45 offen offset:0 sc1\n\t"
      "buffer_load_dwordx4 v[32:35], v12, s[28:31], s45 offen offset:2048 sc1\n\t"
      "buffer_load_dwordx4 v[40:43], v13, s[28:31], s45 offen offset:0 sc1\n\t"
      "buffer_load_dwordx4 v[36:39], v12, s[28:31], s47 offen offset:0 sc1\n\t"
      "buffer_load_dwordx4 v[44:47], v13, s[28:31], s45 offen offset:2048 sc1\n\t"
      "buffer_load_dwordx4 v[48:51], v13, s[28:31], s47 offen offset:0 sc1\n\t"
      "buffer_load_dwordx4 v[52:55], v122, s[28:31], s47 offen offset:2048 sc1\n\t"
      "s_branch L_chk2_%=\n\t"
      "L_sdr_%=:\n\t"
      "s_sub_u32 s46, s46, 1\n\t"
      "s_cbranch_scc1 L_sdt_%=\n\t"
      "buffer_load_dword v119, off, s[28:31], s14 offset:2048 sc1\n\t"
      "s_branch L_red1_%=\n\t"
      "L_sdt_%=:\n\t"
      "s_mov_b32 s46, 0\n\t"
      "s_branch L_red2_%=\n\t"
      "L_redpre_%=:\n\t"
      "buffer_load_dwordx4 v[16:19], v122, s[36:39], 0 offen\n\t"
      "buffer_load_dwordx4 v[20:23], v122, s[36:39], 0 offen offset:1024\n\t"
      "s_lshl_b32 s16, s19, 10\n\t"
      "s_and_b32 s18, s16, 0x3800\n\t"
      "buffer_load_dwordx4 v[8:11], v122, s[24:27], s18 offen\n\t"
      "buffer_load_dwordx4 v[12:15], v122, s[24:27], s18 offen offset:1024\n\t"
      "s_bfe_u32 s16, s19, 0x30004\n\t"
      "s_lshl_b32 s16, s16, 8\n\t"
      "s_add_u32 s18, s18, s16\n\t"
      "s_and_b32 s16, s19, 1\n\t"
      "s_lshl_b32 s16, s16, 3\n\t"
      "s_add_u32 s4, s18, s16\n\t"
      "v_lshrrev_b32 v3, 2, %2\n\t"
      "v_and_or_b32 v123, v3, 4, v4\n\t"
      "v_lshrrev_b32 v3, 4, %2\n\t"
      "v_and_or_b32 v123, v3, 2, v123\n\t"
      "buffer_load_ushort v124, v123, s[24:27], s4 offen\n\t"
      "buffer_load_ushort v118, v123, s[24:27], s4 offen offset:128\n\t"
      "v_mov_b32 v125, 0\n\t"
      "s_waitcnt vmcnt(0)\n\t"
      "v_dot2c_f32_bf16 v125, v8, v16\n\t"
      "v_dot2c_f32_bf16 v125, v9, v17\n\t"
      "v_dot2c_f32_bf16 v125, v10, v18\n\t"
      "v_dot2c_f32_bf16 v125, v11, v19\n\t"
      "v_dot2c_f32_bf16 v125, v12, v20\n\t"
      "v_dot2c_f32_bf16 v125, v13, v21\n\t"
      "v_dot2c_f32_bf16 v125, v14, v22\n\t"
      "v_dot2c_f32_bf16 v125, v15, v23\n\t"
      "s_nop 2\n\t"
      "v_add_f32_dpp v125, v125, v125 row_ror:1 row_mask:0xf bank_mask:0xf\n\t"
      "s_nop 1\n\t"
      "v_add_f32_dpp v125, v125, v125 row_ror:2 row_mask:0xf bank_mask:0xf\n\t"
      "s_nop 1\n\t"
      "v_add_f32_dpp v125, v125, v125 row_ror:4 row_mask:0xf bank_mask:0xf\n\t"
      "s_nop 1\n\t"
      "v_add_f32_dpp v125, v125, v125 row_ror:8 row_mask:0xf bank_mask:0xf\n\t"
      "s_nop 1\n\t"
      "v_mov_b32 v126, v125\n\t"
      "s_nop 1\n\t"
      "v_permlane16_swap_b32 v125, v126\n\t"
      "v_add_f32 v125, v125, v126\n\t"
      "v_mov_b32 v126, v125\n\t"
      "s_nop 1\n\t"
      "v_permlane32_swap_b32 v125, v126\n\t"
      "v_add_f32 v125, v125, v126\n\t"
      "v_cvt_pk_bf16_f32 v125, 0, v125\n\t"
      "v_mul_f32 v125, 0xbfb8aa3b, v125\n\t"
      "v_exp_f32 v125, v125\n\t"
      "s_nop 0\n\t"
      "v_add_f32 v125, 1.0, v125\n\t"
      "v_rcp_f32 v125, v125\n\t"
      "s_nop 0\n\t"
      "v_cvt_pk_bf16_f32 v125, 0, v125\n\t"
      "s_branch L_warmback_%=\n\t"
      "L_probe_%=:\n\t"
      "s_lshr_b32 s16, s6, 2\n\t"
      "s_lshl_b32 s16, s16, 10\n\t"
      "s_add_u32 s16, s16, s14\n\t"
      "s_cmpk_lg_u32 s16, 92\n\t"
      "s_cbranch_scc1 L_epoch_%=\n\t"
      "s_memtime s[36:37]\n\t"
      "s_waitcnt lgkmcnt(0)\n\t"
      "s_load_dword s42, s[28:29], s45\n\t"
      "s_waitcnt lgkmcnt(0)\n\t"
      "s_memtime s[38:39]\n\t"
      "s_waitcnt lgkmcnt(0)\n\t"
      "s_load_dword s42, s[28:29], s45 offset:24960\n\t"
      "s_waitcnt lgkmcnt(0)\n\t"
      "s_memtime s[40:41]\n\t"
      "s_lshl_b32 s38, s38, 1\n\t"
      "s_sub_u32 s38, s38, s36\n\t"
      "s_and_b32 s18, s54, -4\n\t"
      "s_waitcnt lgkmcnt(0)\n\t"
      "s_sub_u32 s38, s38, s40\n\t"
      "s_cmpk_gt_i32 s38, -96\n\t"
      "s_cselect_b32 s16, 2, 0\n\t"
      "s_cmpk_gt_i32 s38, 96\n\t"
      "s_cselect_b32 s16, 1, s16\n\t"
      "s_or_b32 s18, s18, s16\n\t"
      "s_movk_i32 s10, 244\n\t"
      "s_call_b64 s[48:49], L_wait_%=\n\t"
      "v_mov_b32 v8, s18\n\t"
      "v_lshrrev_b32 v9, 2, v122\n\t"
      "v_and_b32 v9, 0x7c, v9\n\t"
      "s_lshl_b32 s16, s6, 7\n\t"
      "buffer_store_dword v8, v9, s[28:31], s16 offen offset:2048 sc1\n\t"
      "s_endpgm\n\t"
      "L_epoch_%=:\n\t"
      "s_add_u32 s16, s19, s5\n\t"
      "s_cmpk_lg_u32 s16, 447\n\t"
      "s_cbranch_scc1 L_done_%=\n\t"
      "s_and_b32 s56, s54, 0xff\n\t"
      "s_and_b32 s57, s50, 0xff\n\t"
      "s_and_b32 s58, s51, 0xff\n\t"
      "s_sub_u32 s56, 0, s56\n\t"
      "s_subb_u32 s57, 0, s57\n\t"
      "s_sub_u32 s58, 0, s58\n\t"
      "s_subb_u32 s59, 0, s59\n\t"
      "v_lshlrev_b32 v9, 7, v122\n\t"
      "s_mov_b32 s46, 1048576\n\t"
      "s_movk_i32 s10, 320\n\t"
      "s_call_b64 s[48:49], L_wait_%=\n\t"
      "L_ep1_%=:\n\t"
      "s_movk_i32 s16, 4096\n\t"
      "buffer_load_dwordx4 v[56:59], v9, s[28:31], s16 offen sc1\n\t"
      "s_add_u32 s16, s16, 0x20000\n\t"
      "buffer_load_dwordx4 v[60:63], v9, s[28:31], s16 offen sc1\n\t"
      "s_add_u32 s16, s16, 0x20000\n\t"
      "buffer_load_dwordx4 v[64:67], v9, s[28:31], s16 offen sc1\n\t"
      "s_add_u32 s16, s16, 0x20000\n\t"
      "buffer_load_dwordx4 v[68:71], v9, s[28:31], s16 offen sc1\n\t"
      "s_waitcnt vmcnt(0)\n\t"
      "v_lshl_add_u64 v[56:57], v[56:57], 0, s[56:57]\n\t"
      "v_lshl_add_u64 v[58:59], v[58:59], 0, s[58:59]\n\t"
      "v_lshl_add_u64 v[60:61], v[60:61], 0, s[56:57]\n\t"
      "v_lshl_add_u64 v[62:63], v[62:63], 0, s[58:59]\n\t"
      "v_lshl_add_u64 v[64:65], v[64:65], 0, s[56:57]\n\t"
      "v_lshl_add_u64 v[66:67], v[66:67], 0, s[58:59]\n\t"
      "v_lshl_add_u64 v[68:69], v[68:69], 0, s[56:57]\n\t"
      "v_lshl_add_u64 v[70:71], v[70:71], 0, s[58:59]\n\t"
      "v_or_b32 v5, v56, v57\n\t"
      "v_or3_b32 v5, v5, v58, v59\n\t"
      "v_or3_b32 v5, v5, v60, v61\n\t"
      "v_or3_b32 v5, v5, v62, v63\n\t"
      "v_or3_b32 v5, v5, v64, v65\n\t"
      "v_or3_b32 v5, v5, v66, v67\n\t"
      "v_or3_b32 v5, v5, v68, v69\n\t"
      "v_or3_b32 v5, v5, v70, v71\n\t"
      "v_and_b32 v5, v5, v117\n\t"
      "v_cmp_ne_u32 vcc, 0, v5\n\t"
      "s_cbranch_vccz L_ep2_%=\n\t"
      "s_sub_u32 s46, s46, 1\n\t"
      "s_cbranch_scc0 L_ep1_%=\n\t"
      "L_ep2_%=:\n\t"
      "v_mov_b32 v8, s54\n\t"
      "s_mov_b64 exec, 1\n\t"
      "buffer_store_dword v8, off, s[28:31], 0 sc1\n\t"
      "s_endpgm\n\t"
      :
      : "s"(__builtin_amdgcn_kernarg_segment_ptr()), "s"(blockIdx.x), "v"(threadIdx.x),
        "s"((unsigned)(size_t)(__attribute__((address_space(3))) unsigned char*)smem)
      : "v1", "v2", "v3", "v4", "v5", "v6", "v7", "v8", "v9", "v10", "v11", "v12", "v13", "v14", "v15", "v16",
        "v17", "v18", "v19", "v20", "v21", "v22", "v23", "v24", "v25", "v26", "v27", "v28", "v29", "v30", "v31", "v32",
        "v33", "v34", "v35", "v36", "v37", "v38", "v39", "v40", "v41", "v42", "v43", "v44", "v45", "v46", "v47", "v48",
        "v49", "v50", "v51", "v52", "v53", "v54", "v55", "v56", "v57", "v58", "v59", "v60", "v61", "v62", "v63", "v64",
        "v65", "v66", "v67", "v68", "v69", "v70", "v71", "v72", "v73", "v74", "v75", "v76", "v77", "v78", "v79", "v80",
        "v81", "v82", "v83", "v84", "v85", "v86", "v87", "v88", "v89", "v90", "v91", "v92", "v93", "v94", "v95", "v96",
        "v97", "v98", "v99", "v100", "v101", "v102", "v103", "v104", "v105", "v106", "v107", "v108", "v109", "v110", "v111", "v112",
        "v113", "v114", "v115", "v116", "v117", "v118", "v119", "v120", "v121", "v122", "v123", "v124", "v125", "v126", "v127", "s4",
        "s5", "s6", "s7", "s8", "s9", "s10", "s11", "s12", "s13", "s14", "s15", "s16", "s17", "s18", "s19", "s20",
        "s21", "s22", "s23", "s24", "s25", "s26", "s27", "s28", "s29", "s30", "s31", "s34", "s35", "s36", "s37", "s38",
        "s39", "s40", "s41", "s42", "s43", "s44", "s45", "s46", "s47", "s48", "s49", "s50", "s51", "s52", "s53", "s54",
        "s55", "s56", "s57", "s58", "s59", "s60", "s61", "s62", "s63", "vcc", "scc", "memory");
}

static inline uint64_t srd_hi(uint32_t bytes) { return ((uint64_t)0x20000u << 32) | bytes; }

extern "C" void kernel_launch(void* const* d_in, const int* in_sizes, int n_in, void* d_out, int out_size, void* d_ws,
                              size_t ws_size, hipStream_t stream) {
  moe_decode<<<256, 512, 0, stream>>>(d_in[2], srd_hi(0x700000), d_in[3], srd_hi(0x700000), d_in[0], srd_hi(0x4000),
                                      d_ws, srd_hi(4194304), d_in[4], srd_hi(0x700000), d_in[1], srd_hi(0x800),
                                      d_out, srd_hi(0x4000));
}
```
